```python
import torch

def _softmax(x, axis=-1):
    return torch.softmax(x, dim=axis)
def _gelu(x):
    return torch.nn.functional.gelu(x, approximate="tanh")
def _silu(x):
    return torch.nn.functional.silu(x)
def _sigmoid(x):
    return torch.sigmoid(x)
def _layernorm(x, axis=-1, eps=1e-5):
    m = x.mean(dim=axis, keepdim=True)
    v = x.var(dim=axis, keepdim=True, unbiased=False)
    return (x - m) / torch.sqrt(v + eps)
def _scatter_add(tgt, idx, src, axis=0):
    return tgt.clone().index_add_(axis, idx, src)
def _segment_sum(src, seg, n):
    out = torch.zeros((n,) + src.shape[1:], dtype=src.dtype)
    return out.index_add_(0, seg, src)
def _pad(x, pw):
    flat = tuple(v for pair in reversed(pw) for v in pair)
    return torch.nn.functional.pad(x, flat)

kernel_name = 'standalone__gqa_block_cached__decode'

def setup_inputs():

    _w = (lambda a: (lambda t: t.to(torch.bfloat16) if t.is_floating_point() else t)(torch.as_tensor(a)))
    B, Sq, D = 2, 1, 1024
    Hq, Hkv, Sk, Dh = 32, 4, 32768, 128
    assert Hq % Hkv == 0, (Hq, Hkv)
    return {
        "x": _w((lambda *s: torch.randn(*s, dtype=torch.bfloat16) * 0.8)(B, Sq, D)),
        "k_cache": _w((lambda *s: torch.randn(*s, dtype=torch.bfloat16) * 0.8)(B, Hkv, Sk, Dh)),
        "v_cache": _w((lambda *s: torch.randn(*s, dtype=torch.bfloat16) * 0.05)(B, Hkv, Sk, Dh)),
        "wq": _w((lambda *s: torch.randn(*s, dtype=torch.bfloat16) * 0.8)(D, Hq * Dh) * (D ** -0.5)),
        "wo": _w((lambda *s: torch.randn(*s, dtype=torch.bfloat16) * 0.05)(Hq * Dh, D) * ((Hq * Dh) ** -0.5)),
    }

def reference(x, k_cache, v_cache, wq, wo):
    B, Sq, D = x.shape
    Hq, Dh = 32, 128
    Hkv = k_cache.shape[1]
    rep = Hq // Hkv
    q = (lambda x, *d: x.permute(*d))((x @ wq).reshape(B, Sq, Hq, Dh), 0, 2, 1, 3)
    k = (lambda x, n, axis: torch.repeat_interleave(x, n, dim=axis))(k_cache, rep, axis=1)
    v = (lambda x, n, axis: torch.repeat_interleave(x, n, dim=axis))(v_cache, rep, axis=1)
    scores = torch.einsum("bhqd,bhkd->bhqk", q, k) * (Dh ** -0.5)
    p = _softmax(scores, axis=-1)
    o = torch.einsum("bhqk,bhkd->bhqd", p, v)
    return (lambda x, *d: x.permute(*d))(o, 0, 2, 1, 3).reshape(B, Sq, Hq * Dh) @ wo
```

```cpp
#include <hip/hip_runtime.h>
#include <hip/hip_bf16.h>
#include <stdint.h>

typedef unsigned v4u __attribute__((ext_vector_type(4)));
typedef unsigned v2u __attribute__((ext_vector_type(2)));
typedef float f32x4 __attribute__((ext_vector_type(4)));
typedef short bf16x8 __attribute__((ext_vector_type(8)));
#define GAS __attribute__((address_space(1)))

constexpr int B = 2, D = 1024, HQ = 32, HKV = 4, SK = 32768, DH = 128;
constexpr int NWG = 256, NTHR = 512, NWAVE = 8;
constexpr int KEYS_PER_WG = SK / 32;
constexpr float C2 = 0.08838834764831845f * 1.4426950408889634f;

__device__ __forceinline__ v4u ldnt(const void* p) { return __builtin_nontemporal_load((const GAS v4u*)p); }
__device__ __forceinline__ v4u ldp(const void* p) { return *(const GAS v4u*)p; }
__device__ __forceinline__ f32x4 mfma(v4u a, v4u b, f32x4 c) {
  return __builtin_amdgcn_mfma_f32_16x16x32_bf16(__builtin_bit_cast(bf16x8, a), __builtin_bit_cast(bf16x8, b), c, 0, 0, 0);
}
__device__ __forceinline__ float bflo(unsigned u) { return __builtin_bit_cast(float, u << 16); }
__device__ __forceinline__ float bfhi(unsigned u) { return __builtin_bit_cast(float, u & 0xffff0000u); }
typedef __bf16 bf16x2_t __attribute__((ext_vector_type(2)));
typedef float f32x2 __attribute__((ext_vector_type(2)));
__device__ __forceinline__ unsigned cvtpk(float lo, float hi) {
  f32x2 v = {lo, hi};
  bf16x2_t r = __builtin_convertvector(v, bf16x2_t);
  return __builtin_bit_cast(unsigned, r);
}

constexpr size_t WS_Q = 0;
constexpr size_t WS_PO = 65536;
constexpr size_t WS_PML = WS_PO + 256 * 1024 * 4;
constexpr size_t WS_P2 = WS_PML + 256 * 16 * 4;

__global__ void __launch_bounds__(NTHR) k_qproj(const unsigned short* x, const unsigned short* wq, unsigned char* ws) {
  __shared__ float red[NWAVE][32];
  const int w = blockIdx.x, t = threadIdx.x, lane = t & 63, wv = t >> 6;
  const int cg = (w & 7) * 32 + (w >> 3);
  const int half = t & 1, rg = t >> 1;
  v4u wv4[4];
  float x0[4], x1[4];
#pragma unroll
  for (int i = 0; i < 4; ++i) {
    const int row = rg + 256 * i;
    wv4[i] = ldp(wq + (size_t)row * 4096 + cg * 16 + half * 8);
    x0[i] = bflo(x[row]);
    x1[i] = bflo(x[1024 + row]);
  }
  float acc[16];
#pragma unroll
  for (int j = 0; j < 16; ++j) acc[j] = 0.f;
#pragma unroll
  for (int i = 0; i < 4; ++i)
#pragma unroll
    for (int u = 0; u < 4; ++u) {
      const float a = bflo(wv4[i][u]), b = bfhi(wv4[i][u]);
      acc[2 * u] += x0[i] * a; acc[2 * u + 1] += x0[i] * b;
      acc[8 + 2 * u] += x1[i] * a; acc[8 + 2 * u + 1] += x1[i] * b;
    }
#pragma unroll
  for (int j = 0; j < 16; ++j)
#pragma unroll
    for (int o = 2; o < 64; o <<= 1) acc[j] += __shfl_xor(acc[j], o);
  if (lane < 2) {
#pragma unroll
    for (int j = 0; j < 16; ++j) red[wv][(j >> 3) * 16 + lane * 8 + (j & 7)] = acc[j];
  }
  __syncthreads();
  if (t < 16) {
    float s0 = 0.f, s1 = 0.f;
#pragma unroll
    for (int ww = 0; ww < NWAVE; ++ww) { s0 += red[ww][2 * t]; s1 += red[ww][2 * t + 1]; }
    unsigned* q = (unsigned*)(ws + WS_Q);
    q[(t >> 3) * 2048 + cg * 8 + (t & 7)] = cvtpk(s0, s1);
  }
}

__global__ void __launch_bounds__(NTHR, 2) k_attn(const unsigned short* kc, const unsigned short* vc, unsigned char* ws) {
  __shared__ __attribute__((aligned(16))) float red[NWAVE][8][128];
  __shared__ float rml[NWAVE][8][2];
  const int w = blockIdx.x, t = threadIdx.x, lane = t & 63, wv = __builtin_amdgcn_readfirstlane(t >> 6);
  const int p = w & 7, s = w >> 3, b = p >> 2, kvh = p & 3;
  const int a = lane & 15, g = lane >> 4;
  const size_t base = ((size_t)p * SK + (size_t)s * KEYS_PER_WG) * DH;
  const unsigned char* kb = (const unsigned char*)(kc + base) + (size_t)wv * 8192 + (4 * (a & 3) + (a >> 2)) * 256 + g * 16;
  const unsigned char* vb = (const unsigned char*)(vc + base) + (size_t)wv * 8192 + lane * 16;
  v4u R[4][8];
#pragma unroll
  for (int i = 0; i < 4; ++i)
#pragma unroll
    for (int u = 0; u < 2; ++u)
#pragma unroll
      for (int st = 0; st < 4; ++st) R[i][u * 4 + st] = ldnt(kb + i * 65536 + u * 4096 + st * 64);
  __builtin_amdgcn_sched_barrier(0);
  v4u qf[4];
  const unsigned char* qb = ws + WS_Q + ((size_t)b * 4096 + kvh * 1024 + (a & 7) * 128) * 2 + g * 16;
#pragma unroll
  for (int st = 0; st < 4; ++st) qf[st] = ldp(qb + st * 64);
  f32x4 S[4][2];
#pragma unroll
  for (int i = 0; i < 4; ++i) {
#pragma unroll
    for (int u = 0; u < 2; ++u) {
      f32x4 c = {0.f, 0.f, 0.f, 0.f};
#pragma unroll
      for (int st = 0; st < 4; ++st) c = mfma(R[i][u * 4 + st], qf[st], c);
      S[i][u] = c;
    }
    __builtin_amdgcn_sched_barrier(0);
#pragma unroll
    for (int j = 0; j < 8; ++j) R[i][j] = ldnt(vb + i * 65536 + j * 1024);
    __builtin_amdgcn_sched_barrier(0);
  }
  float m = -INFINITY;
#pragma unroll
  for (int i = 0; i < 4; ++i)
#pragma unroll
    for (int u = 0; u < 2; ++u)
#pragma unroll
      for (int r = 0; r < 4; ++r) m = fmaxf(m, S[i][u][r]);
  m = fmaxf(m, __shfl_xor(m, 16));
  m = fmaxf(m, __shfl_xor(m, 32));
  const float mc = m * C2;
  float l = 0.f;
  v4u P[4];
#pragma unroll
  for (int i = 0; i < 4; ++i) {
    float e[8];
#pragma unroll
    for (int u = 0; u < 2; ++u)
#pragma unroll
      for (int r = 0; r < 4; ++r) { e[u * 4 + r] = __builtin_amdgcn_exp2f(S[i][u][r] * C2 - mc); l += e[u * 4 + r]; }
    P[i] = v4u{cvtpk(e[0], e[1]), cvtpk(e[2], e[3]), cvtpk(e[4], e[5]), cvtpk(e[6], e[7])};
  }
  l += __shfl_xor(l, 16);
  l += __shfl_xor(l, 32);
  f32x4 O[8];
#pragma unroll
  for (int e = 0; e < 8; ++e) O[e] = f32x4{0.f, 0.f, 0.f, 0.f};
#pragma unroll
  for (int i = 0; i < 4; ++i) {
#pragma unroll
    for (int e = 0; e < 8; ++e) {
      const unsigned sel = (e & 1) ? 0x07060302u : 0x05040100u;
      v4u bv;
#pragma unroll
      for (int u = 0; u < 4; ++u) bv[u] = __builtin_amdgcn_perm(R[i][2 * u + 1][e >> 1], R[i][2 * u][e >> 1], sel);
      O[e] = mfma(P[i], bv, O[e]);
    }
    __builtin_amdgcn_sched_barrier(0);
  }
  if (g < 2) {
#pragma unroll
    for (int r = 0; r < 4; ++r) {
      *(f32x4*)&red[wv][4 * g + r][8 * a] = f32x4{O[0][r], O[1][r], O[2][r], O[3][r]};
      *(f32x4*)&red[wv][4 * g + r][8 * a + 4] = f32x4{O[4][r], O[5][r], O[6][r], O[7][r]};
    }
  }
  if (lane < 8) { rml[wv][lane][0] = mc; rml[wv][lane][1] = l; }
  __syncthreads();
  {
    const int h = wv, d = 2 * lane;
    float M = -INFINITY;
#pragma unroll
    for (int ww = 0; ww < NWAVE; ++ww) M = fmaxf(M, rml[ww][h][0]);
    float L = 0.f, o0 = 0.f, o1 = 0.f;
#pragma unroll
    for (int ww = 0; ww < NWAVE; ++ww) {
      const float wt = __builtin_amdgcn_exp2f(rml[ww][h][0] - M);
      L += wt * rml[ww][h][1];
      o0 += wt * red[ww][h][d];
      o1 += wt * red[ww][h][d + 1];
    }
    float* po = (float*)(ws + WS_PO) + (size_t)w * 1024 + h * 128 + d;
    *(float2*)po = make_float2(o0, o1);
    if (lane == 0) { float* pml = (float*)(ws + WS_PML) + w * 16 + h * 2; pml[0] = M; pml[1] = L; }
  }
}

__global__ void __launch_bounds__(NTHR) k_comb(const unsigned short* wo, unsigned char* ws) {
  __shared__ float ov[2][16];
  __shared__ float part[2][32][17];
  const int cg = blockIdx.x, t = threadIdx.x;
  const int hg = cg >> 3, kvh = hg >> 3, h = hg & 7, d0 = (cg & 7) * 16;
  v4u wv4[4];
#pragma unroll
  for (int i = 0; i < 4; ++i) wv4[i] = ldp(wo + (size_t)(cg * 16 + (t >> 7) * 4 + i) * 1024 + (t & 127) * 8);
  const float* po = (const float*)(ws + WS_PO);
  const float* pml = (const float*)(ws + WS_PML);
#pragma unroll
  for (int b = 0; b < 2; ++b) {
    const int s = t >> 4, dd = t & 15;
    const int p = b * 4 + kvh;
    float M = -INFINITY;
    for (int ss = 0; ss < 32; ++ss) M = fmaxf(M, pml[(ss * 8 + p) * 16 + h * 2]);
    float L = 0.f;
    for (int ss = 0; ss < 32; ++ss) L += __builtin_amdgcn_exp2f(pml[(ss * 8 + p) * 16 + h * 2] - M) * pml[(ss * 8 + p) * 16 + h * 2 + 1];
    const float wt = __builtin_amdgcn_exp2f(pml[(s * 8 + p) * 16 + h * 2] - M) / L;
    part[b][s][dd] = wt * po[(size_t)(s * 8 + p) * 1024 + h * 128 + d0 + dd];
  }
  __syncthreads();
  if (t < 32) {
    float sum = 0.f;
    for (int ss = 0; ss < 32; ++ss) sum += part[t >> 4][ss][t & 15];
    ov[t >> 4][t & 15] = bflo(cvtpk(sum, 0.f) & 0xffffu);
  }
  __syncthreads();
  __shared__ float acc2[4][2][1024];
  float a0[8], a1[8];
#pragma unroll
  for (int j = 0; j < 8; ++j) { a0[j] = 0.f; a1[j] = 0.f; }
#pragma unroll
  for (int i = 0; i < 4; ++i) {
    const float o0 = ov[0][(t >> 7) * 4 + i], o1 = ov[1][(t >> 7) * 4 + i];
#pragma unroll
    for (int u = 0; u < 4; ++u) {
      const float wa = bflo(wv4[i][u]), wb = bfhi(wv4[i][u]);
      a0[2 * u] += o0 * wa; a0[2 * u + 1] += o0 * wb;
      a1[2 * u] += o1 * wa; a1[2 * u + 1] += o1 * wb;
    }
  }
#pragma unroll
  for (int j = 0; j < 8; ++j) { acc2[t >> 7][0][(t & 127) * 8 + j] = a0[j]; acc2[t >> 7][1][(t & 127) * 8 + j] = a1[j]; }
  __syncthreads();
  float* p2 = (float*)(ws + WS_P2) + (size_t)cg * 2048;
#pragma unroll
  for (int i = 0; i < 4; ++i) {
    const int idx = t + 512 * i;
    const float* a = &acc2[0][0][0];
    p2[idx] = (a[idx] + a[2048 + idx]) + (a[4096 + idx] + a[6144 + idx]);
  }
}

__global__ void __launch_bounds__(256) k_final(unsigned char* ws, unsigned short* out) {
  __shared__ float r[32][9];
  const int w = blockIdx.x, t = threadIdx.x;
  const float* p2 = (const float*)(ws + WS_P2);
  const int j = t & 7, sg = t >> 3;
  float s = 0.f;
#pragma unroll
  for (int i = 0; i < 8; ++i) s += p2[(size_t)(sg * 8 + i) * 2048 + w * 8 + j];
  r[sg][j] = s;
  __syncthreads();
  if (t < 8) {
    float tot = 0.f;
    for (int i = 0; i < 32; ++i) tot += r[i][t];
    out[w * 8 + t] = (unsigned short)(cvtpk(tot, 0.f) & 0xffffu);
  }
}

extern "C" void kernel_launch(void* const* d_in, const int* in_sizes, int n_in, void* d_out, int out_size, void* d_ws,
                              size_t ws_size, hipStream_t stream) {
  const unsigned short* x = (const unsigned short*)d_in[0];
  const unsigned short* kc = (const unsigned short*)d_in[1];
  const unsigned short* vc = (const unsigned short*)d_in[2];
  const unsigned short* wq = (const unsigned short*)d_in[3];
  const unsigned short* wo = (const unsigned short*)d_in[4];
  unsigned char* ws = (unsigned char*)d_ws;
  k_qproj<<<NWG, NTHR, 0, stream>>>(x, wq, ws);
  k_attn<<<NWG, NTHR, 0, stream>>>(kc, vc, ws);
  k_comb<<<NWG, NTHR, 0, stream>>>(wo, ws);
  k_final<<<256, 256, 0, stream>>>(ws, (unsigned short*)d_out);
}
```

```cpp
#include <hip/hip_runtime.h>
#include <hip/hip_bf16.h>
#include <stdint.h>

typedef unsigned v4u __attribute__((ext_vector_type(4)));
typedef unsigned v2u __attribute__((ext_vector_type(2)));
typedef float f32x4 __attribute__((ext_vector_type(4)));
typedef float f32x2 __attribute__((ext_vector_type(2)));
typedef short bf16x8 __attribute__((ext_vector_type(8)));
typedef __bf16 bf16x2_t __attribute__((ext_vector_type(2)));
#define GAS __attribute__((address_space(1)))
#define LAS __attribute__((address_space(3)))
typedef GAS unsigned gu32;
typedef unsigned long long u64;
#define RLX_AGENT __ATOMIC_RELAXED, __HIP_MEMORY_SCOPE_AGENT
#define RLX_WG __ATOMIC_RELAXED, __HIP_MEMORY_SCOPE_WORKGROUP

constexpr int SK = 32768, DH = 128;
constexpr int NWG = 256, NTHR = 512, NWAVE = 8;
constexpr float C2 = 0.08838834764831845f * 1.4426950408889634f;
constexpr unsigned kSpinCap = 1u << 17;

#define PACE 2
#define PACEV PACE
#define NPRE 4
#define QPOLL_AT 10
#define WOPOS 2
#define QLAG 1
#define NAP 1
#define STR_(x) #x
#define STR(x) STR_(x)
#define PACEWAIT() asm volatile("s_waitcnt vmcnt(" STR(PACE) ")" ::: "memory")
#define PACEWAITV() asm volatile("s_waitcnt vmcnt(" STR(PACEV) ")" ::: "memory")
#define PIN() do { asm volatile("" ::: "memory"); __builtin_amdgcn_sched_barrier(0); } while (0)

constexpr int WS_EPOCH = 0;
constexpr int WS_QP = 4096;
constexpr int WS_PO = WS_QP + 256 * 512;
constexpr int PO_STR = 4096 + 128;
constexpr int WS_P2 = WS_PO + 256 * PO_STR;
constexpr int WS_END = WS_P2 + 256 * 4096;
#define STAMP(k) do {} while (0)

__device__ __forceinline__ v4u ldnt(const void* p) { return __builtin_nontemporal_load((const GAS v4u*)p); }
__device__ __forceinline__ v2u ldnt64(const void* p) { return __builtin_nontemporal_load((const GAS v2u*)p); }
__device__ __forceinline__ f32x4 mfma(v4u a, v4u b, f32x4 c) {
  return __builtin_amdgcn_mfma_f32_16x16x32_bf16(__builtin_bit_cast(bf16x8, a), __builtin_bit_cast(bf16x8, b), c, 0, 0, 0);
}
__device__ __forceinline__ unsigned fbits(float f) { return __builtin_bit_cast(unsigned, f); }
__device__ __forceinline__ float bitsf(unsigned u) { return __builtin_bit_cast(float, u); }
__device__ __forceinline__ float bflo(unsigned u) { return bitsf(u << 16); }
__device__ __forceinline__ float bfhi(unsigned u) { return bitsf(u & 0xffff0000u); }
__device__ __forceinline__ unsigned cvtpk(float lo, float hi) {
  f32x2 v = {lo, hi};
  return __builtin_bit_cast(unsigned, __builtin_convertvector(v, bf16x2_t));
}
template <int CTRL> __device__ __forceinline__ float dpp(float x) {
  return bitsf(__builtin_amdgcn_mov_dpp((int)fbits(x), CTRL, 0xf, 0xf, true));
}
constexpr int ROR8 = 0x128;
__device__ __forceinline__ float swap32_add(float x, float y) {
  auto r = __builtin_amdgcn_permlane32_swap(fbits(x), fbits(y), false, false);
  return bitsf(r[0]) + bitsf(r[1]);
}
__device__ __forceinline__ float swap16_add(float x, float y) {
  auto r = __builtin_amdgcn_permlane16_swap(fbits(x), fbits(y), false, false);
  return bitsf(r[0]) + bitsf(r[1]);
}
__device__ __forceinline__ float swap32_max(float x) {
  auto r = __builtin_amdgcn_permlane32_swap(fbits(x), fbits(x), false, false);
  return fmaxf(bitsf(r[0]), bitsf(r[1]));
}
__device__ __forceinline__ float swap16_max(float x) {
  auto r = __builtin_amdgcn_permlane16_swap(fbits(x), fbits(x), false, false);
  return fmaxf(bitsf(r[0]), bitsf(r[1]));
}
__device__ __forceinline__ float red4_hi(const float (&v)[4]) {
  const float u0 = swap32_add(v[0], v[2]), u1 = swap32_add(v[1], v[3]);
  const float z = swap16_add(u0, u1);
  return z + dpp<ROR8>(z);
}
__device__ __forceinline__ v4u tag4(float a, float b, float c, float d, unsigned tag) {
  return v4u{__builtin_amdgcn_perm(fbits(a), tag, 0x07060500u), __builtin_amdgcn_perm(fbits(b), tag, 0x07060501u),
             __builtin_amdgcn_perm(fbits(c), tag, 0x07060502u), __builtin_amdgcn_perm(fbits(d), tag, 0x07060503u)};
}
__device__ __forceinline__ bool ok4(v4u x, unsigned tag) {
  const unsigned lo = __builtin_amdgcn_perm(x[1], x[0], 0x0c0c0400u), hi = __builtin_amdgcn_perm(x[3], x[2], 0x04000c0cu);
  return (lo | hi) == tag;
}
__device__ __forceinline__ float val(unsigned x) { return bitsf(x & 0xffffff00u); }
__device__ __forceinline__ void put4(__amdgpu_buffer_rsrc_t rs, int off, v4u v) { __builtin_amdgcn_raw_buffer_store_b128(v, rs, off, 0, 16); }
__device__ __forceinline__ v4u get4(__amdgpu_buffer_rsrc_t rs, int off) { return __builtin_amdgcn_raw_buffer_load_b128(rs, off, 0, 16); }
template <int N> __device__ __forceinline__ void settle(__amdgpu_buffer_rsrc_t rs, const int (&off)[N], v4u (&x)[N], unsigned tag) {
  bool ok = true;
#pragma unroll
  for (int k = 0; k < N; ++k) ok &= ok4(x[k], tag);
  if (__builtin_expect(!__all(ok), 0)) {
#pragma unroll 1
    for (unsigned spins = 0; spins < kSpinCap; ++spins) {
      __builtin_amdgcn_s_sleep(NAP);
      if (!ok) {
#pragma unroll
        for (int k = 0; k < N; ++k) x[k] = get4(rs, off[k]);
        ok = true;
#pragma unroll
        for (int k = 0; k < N; ++k) ok &= ok4(x[k], tag);
      }
      if (__all(ok)) break;
    }
  }
}
__device__ __forceinline__ bool last_wave(unsigned* cnt, int lane) {
  unsigned old = 0;
  asm volatile("s_waitcnt lgkmcnt(0)" ::: "memory");
  if (lane == 0) old = __hip_atomic_fetch_add((LAS unsigned*)cnt, 1u, RLX_WG);
  const bool last = __builtin_amdgcn_readfirstlane(old) == NWAVE - 1;
  asm volatile("" ::: "memory");
  return last;
}

struct Lds {
  float red[NWAVE][8][128];
  float qred[NWAVE][128];
  unsigned qs[512];
  float rml[NWAVE][8][2];
  float fin[NWAVE][32];
  unsigned short ov[2][32];
  unsigned qready, qcnt, mlcnt;
};

__global__ void __launch_bounds__(NTHR, 2)
k_fused(const unsigned short* __restrict__ x, const unsigned short* __restrict__ kc, const unsigned short* __restrict__ vc,
        const unsigned short* __restrict__ wq, const unsigned short* __restrict__ wo, unsigned short* __restrict__ out, unsigned char* ws) {
  extern __shared__ __attribute__((aligned(16))) unsigned char smem[];
  Lds& L = *(Lds*)smem;
  STAMP(0);
  const int w = blockIdx.x, t = threadIdx.x, lane = t & 63, wv = __builtin_amdgcn_readfirstlane(t >> 6);
  const int p = w & 7, s = w >> 3, b = p >> 2, kvh = p & 3;
  const int id = (w & 7) * 32 + (w >> 3), cgq = id >> 2, kq = id & 3;
  const int a = lane & 15, g = lane >> 4;
  const __amdgpu_buffer_rsrc_t rs = __builtin_amdgcn_make_buffer_rsrc((void*)ws, 0, WS_END, 0x00020000);

  int vzero;
  asm volatile("v_mov_b32 %0, 0" : "=v"(vzero));
  const unsigned epoch = __builtin_amdgcn_raw_buffer_load_b32(rs, WS_EPOCH + vzero, 0, 16);
  const int r0 = 256 * kq + 32 * wv + 4 * (lane >> 3);
  const v2u xa = *(const GAS v2u*)(x + r0), xb = *(const GAS v2u*)(x + 1024 + r0);
  v4u W[4];
#pragma unroll
  for (int i = 0; i < 4; ++i) W[i] = ldnt(wq + (size_t)(r0 + i) * 4096 + 64 * cgq + 8 * (lane & 7));
  if (t == 0) { L.qready = 0u; L.qcnt = 0u; L.mlcnt = 0u; }
  PIN();
  __syncthreads();
  PIN();
  const size_t base = ((size_t)p * SK + (size_t)s * 1024) * (DH * 2);
  const int kap = a >> 1;
  const unsigned char* kb = (const unsigned char*)kc + base + (size_t)wv * 8192 + (4 * (kap & 1) + (kap >> 1)) * 256 + (2 * g + (a & 1)) * 16;
  const unsigned char* vb = (const unsigned char*)vc + base + (size_t)wv * 8192 + lane * 16;
  v4u R[4][8];
#define ISSUE_K(n) do { R[(n) >> 2][2 * ((n) & 3)] = ldnt(kb + ((n) >> 2) * 65536 + ((n) & 3) * 2048); \
    R[(n) >> 2][2 * ((n) & 3) + 1] = ldnt(kb + ((n) >> 2) * 65536 + ((n) & 3) * 2048 + 128); PIN(); } while (0)
#define ISSUE_V(n) do { R[(n) >> 2][2 * ((n) & 3)] = ldnt(vb + ((n) >> 2) * 65536 + ((n) & 3) * 2048); \
    R[(n) >> 2][2 * ((n) & 3) + 1] = ldnt(vb + ((n) >> 2) * 65536 + ((n) & 3) * 2048 + 1024); PIN(); } while (0)
#pragma unroll
  for (int n = 0; n < NPRE; ++n) ISSUE_K(n);

  {
    float acc[2][8];
#pragma unroll
    for (int j = 0; j < 8; ++j) { acc[0][j] = 0.f; acc[1][j] = 0.f; }
#pragma unroll
    for (int i = 0; i < 4; ++i) {
      const float x0 = (i & 1) ? bfhi(xa[i >> 1]) : bflo(xa[i >> 1]);
      const float x1 = (i & 1) ? bfhi(xb[i >> 1]) : bflo(xb[i >> 1]);
#pragma unroll
      for (int u = 0; u < 4; ++u) {
        const float wa = bflo(W[i][u]), wb = bfhi(W[i][u]);
        acc[0][2 * u] += x0 * wa; acc[0][2 * u + 1] += x0 * wb;
        acc[1][2 * u] += x1 * wa; acc[1][2 * u + 1] += x1 * wb;
      }
    }
    float v8[8], v4[4], v2[2];
#pragma unroll
    for (int j = 0; j < 8; ++j) v8[j] = swap32_add(acc[0][j], acc[1][j]);
#pragma unroll
    for (int j = 0; j < 4; ++j) v4[j] = swap16_add(v8[j], v8[j + 4]);
    const bool b3 = lane & 8;
#pragma unroll
    for (int j = 0; j < 2; ++j) {
      const float keep = b3 ? v4[j + 2] : v4[j], send = b3 ? v4[j] : v4[j + 2];
      v2[j] = keep + dpp<ROR8>(send);
    }
    const int col = 8 * (lane & 7) + 4 * ((lane >> 4) & 1) + 2 * ((lane >> 3) & 1);
    *(f32x2*)&L.qred[wv][(lane >> 5) * 64 + col] = f32x2{v2[0], v2[1]};
  }
  STAMP(1);
  const unsigned tag = ((((epoch & 0x00ff00ffu) * 5u + 0x00350047u) & 0x00ff00ffu) | ((((epoch >> 8) & 0x00ff00ffu) * 5u + 0x00a9001du) & 0x00ff00ffu) << 8);
  if (last_wave(&L.qcnt, lane)) {
    if (lane < 32) {
      f32x4 sum = *(const f32x4*)&L.qred[0][4 * lane];
#pragma unroll
      for (int ww = 1; ww < NWAVE; ++ww) sum += *(const f32x4*)&L.qred[ww][4 * lane];
      put4(rs, WS_QP + id * 512 + lane * 16, tag4(sum[0], sum[1], sum[2], sum[3], tag));
    }
  }
  PIN();
  STAMP(2);
  const int oq = WS_QP + ((16 * kvh + 2 * wv + (lane >> 5)) * 4 + 2 * (g & 1)) * 512 + b * 256 + a * 16;
  const int offq[2] = {oq, oq + 512};
  v4u xq[2];
#pragma unroll
  for (int n = NPRE; n < 16; ++n) {
    PACEWAIT(); ISSUE_K(n);
    if (n == QPOLL_AT) { xq[0] = get4(rs, offq[0]); xq[1] = get4(rs, offq[1]); PIN(); }
    if (n == QPOLL_AT + QLAG) {
      settle<2>(rs, offq, xq, tag);
      float q4[4];
#pragma unroll
      for (int e = 0; e < 4; ++e) { const float v = val(xq[0][e]) + val(xq[1][e]); q4[e] = swap16_add(v, v); }
      if (!(g & 1)) *(v2u*)&L.qs[wv * 64 + (lane >> 5) * 32 + 2 * a] = v2u{cvtpk(q4[0], q4[1]), cvtpk(q4[2], q4[3])};
      asm volatile("s_waitcnt lgkmcnt(0)" ::: "memory");
      if (lane == 0) __hip_atomic_fetch_add((LAS unsigned*)&L.qready, 1u, RLX_WG);
      PIN();
      STAMP(3);
    }
  }
  while (__hip_atomic_load((LAS unsigned*)&L.qready, RLX_WG) != NWAVE) __builtin_amdgcn_s_sleep(1);
  PIN();
  STAMP(4);
  v4u qf[2];
#pragma unroll
  for (int dh = 0; dh < 2; ++dh) qf[dh] = *(const v4u*)&L.qs[(a & 7) * 64 + 32 * dh + 8 * g + 4 * (a >> 3)];

  float S[4][8];
  const bool cp = a & 8;
#pragma unroll
  for (int i = 0; i < 4; ++i) {
#pragma unroll
    for (int gm = 0; gm < 4; ++gm) {
      f32x4 c = {0.f, 0.f, 0.f, 0.f};
      c = mfma(R[i][2 * gm], qf[0], c);
      c = mfma(R[i][2 * gm + 1], qf[1], c);
      const float u0 = cp ? c[1] : c[0], u1 = cp ? c[3] : c[2];
      S[i][2 * gm] = u0 + dpp<ROR8>(u0);
      S[i][2 * gm + 1] = u1 + dpp<ROR8>(u1);
    }
    PIN();
    if (i == 0) { ISSUE_V(0); ISSUE_V(1); }
    if (i == 3) { ISSUE_V(2); ISSUE_V(3); }
  }
  STAMP(5);
  float m = S[0][0];
#pragma unroll
  for (int i = 0; i < 4; ++i)
#pragma unroll
    for (int j = 0; j < 8; ++j) m = fmaxf(m, S[i][j]);
  m = swap16_max(m);
  m = swap32_max(m);
  const float mc = m * C2;
  float l = 0.f;
  v4u P[4];
#pragma unroll
  for (int i = 0; i < 4; ++i) {
    float e[8];
#pragma unroll
    for (int j = 0; j < 8; ++j) { e[j] = __builtin_amdgcn_exp2f(S[i][j] * C2 - mc); l += e[j]; }
    P[i] = v4u{cvtpk(e[0], e[1]), cvtpk(e[2], e[3]), cvtpk(e[4], e[5]), cvtpk(e[6], e[7])};
  }
  l = swap16_add(l, l);
  l = swap32_add(l, l);
  if (lane < 8) { L.rml[wv][lane][0] = mc; L.rml[wv][lane][1] = l; }
  if (last_wave(&L.mlcnt, lane)) {
    if (lane < 8) {
      float M = L.rml[0][lane][0];
#pragma unroll
      for (int ww = 1; ww < NWAVE; ++ww) M = fmaxf(M, L.rml[ww][lane][0]);
      M = val(fbits(M));
      float Ls = 0.f;
#pragma unroll
      for (int ww = 0; ww < NWAVE; ++ww) Ls += __builtin_amdgcn_exp2f(L.rml[ww][lane][0] - M) * L.rml[ww][lane][1];
      put4(rs, WS_PO + w * PO_STR + 4096 + lane * 16, tag4(M, Ls, M, Ls, tag));
    }
  }
  PIN();
  f32x4 O[8];
#pragma unroll
  for (int e = 0; e < 8; ++e) O[e] = f32x4{0.f, 0.f, 0.f, 0.f};
  v2u WO[8];
  const int po0 = WS_PO + ((lane >> 3) * 8 + (wv & 1) * 4 + (id >> 6)) * PO_STR;
  const int om = po0 + 4096 + ((id >> 3) & 7) * 16;
  const int offm[4] = {om, om + 64 * PO_STR, om + 128 * PO_STR, om + 192 * PO_STR};
  v4u ML[4] = {};
#pragma unroll
  for (int i = 0; i < 4; ++i) {
    if (i == 0) { PACEWAITV(); ISSUE_V(4); PACEWAITV(); ISSUE_V(5); PACEWAITV(); ISSUE_V(6); }
    if (i == 1) { PACEWAITV(); ISSUE_V(7); PACEWAITV(); ISSUE_V(8); PACEWAITV(); ISSUE_V(9); PACEWAITV(); ISSUE_V(10); }
    if (i == 2) {
      PACEWAITV(); ISSUE_V(11); PACEWAITV(); ISSUE_V(12);
      if (wv < 2) {
#pragma unroll
        for (int k = 0; k < 4; ++k) ML[k] = get4(rs, offm[k]);
      }
      PIN();
      PACEWAITV(); ISSUE_V(13); PACEWAITV(); ISSUE_V(14); PACEWAITV(); ISSUE_V(15);
    }
#pragma unroll
    for (int e = 0; e < 8; ++e) {
      const unsigned sel = (e & 1) ? 0x07060302u : 0x05040100u;
      v4u bv;
#pragma unroll
      for (int u = 0; u < 4; ++u) bv[u] = __builtin_amdgcn_perm(R[i][2 * u + 1][e >> 1], R[i][2 * u][e >> 1], sel);
      O[e] = mfma(P[i], bv, O[e]);
    }
    PIN();
  }
  STAMP(6);
  if (g < 2) {
#pragma unroll
    for (int r = 0; r < 4; ++r) {
      *(f32x4*)&L.red[wv][4 * g + r][8 * a] = f32x4{O[0][r], O[1][r], O[2][r], O[3][r]};
      *(f32x4*)&L.red[wv][4 * g + r][8 * a + 4] = f32x4{O[4][r], O[5][r], O[6][r], O[7][r]};
    }
  }
  __syncthreads();
  if (lane < 32) {
    const int h = wv;
    float M = L.rml[0][h][0];
#pragma unroll
    for (int ww = 1; ww < NWAVE; ++ww) M = fmaxf(M, L.rml[ww][h][0]);
    M = val(fbits(M));
    f32x4 o = {0.f, 0.f, 0.f, 0.f};
#pragma unroll
    for (int ww = 0; ww < NWAVE; ++ww) o += __builtin_amdgcn_exp2f(L.rml[ww][h][0] - M) * *(const f32x4*)&L.red[ww][h][4 * lane];
    put4(rs, WS_PO + w * PO_STR + h * 512 + lane * 16, tag4(o[0], o[1], o[2], o[3], tag));
  }
  PIN();
  STAMP(7);
  const int oo = po0 + ((id >> 3) & 7) * 512 + ((id >> 1) & 3) * 128 + (lane & 7) * 16;
  const int off[4] = {oo, oo + 64 * PO_STR, oo + 128 * PO_STR, oo + 192 * PO_STR};
  v4u xx[4] = {};
  if (wv < 2) {
#pragma unroll
    for (int k = 0; k < 4; ++k) xx[k] = get4(rs, off[k]);
  }
  PIN();
#pragma unroll
  for (int j = 0; j < 8; ++j) WO[j] = ldnt64(wo + (size_t)(32 * (id >> 1) + 8 * g + j) * 1024 + 512 * (id & 1) + 64 * wv + 4 * a);
  PIN();
  if (wv < 2) {
    float wt[4];
    settle<4>(rs, offm, ML, tag);
    float M = fmaxf(fmaxf(val(ML[0][0]), val(ML[1][0])), fmaxf(val(ML[2][0]), val(ML[3][0])));
    M = fmaxf(M, dpp<ROR8>(M));
    M = swap16_max(M); M = swap32_max(M);
    float Ls = 0.f;
#pragma unroll
    for (int k = 0; k < 4; ++k) { wt[k] = __builtin_amdgcn_exp2f(val(ML[k][0]) - M); Ls += wt[k] * val(ML[k][1]); }
    Ls = swap32_add(Ls, Ls); Ls = swap16_add(Ls, Ls); Ls += dpp<ROR8>(Ls);
    const float inv = 1.0f / Ls;
#pragma unroll
    for (int k = 0; k < 4; ++k) { wt[k] *= inv; asm volatile("" : "+v"(wt[k])); }
    PIN();
    settle<4>(rs, off, xx, tag);
    float o[4];
#pragma unroll
    for (int e = 0; e < 4; ++e) o[e] = (wt[0] * val(xx[0][e]) + wt[1] * val(xx[1][e])) + (wt[2] * val(xx[2][e]) + wt[3] * val(xx[3][e]));
    const unsigned pk = cvtpk(red4_hi(o), 0.f);
    if ((lane & 8) == 0) L.ov[wv][4 * (lane & 7) + 2 * (lane >> 5) + ((lane >> 4) & 1)] = (unsigned short)pk;
  }
  PIN();
  v4u BW[4];
#pragma unroll
  for (int e = 0; e < 4; ++e)
#pragma unroll
    for (int u = 0; u < 4; ++u) BW[e][u] = __builtin_amdgcn_perm(WO[2 * u + 1][e >> 1], WO[2 * u][e >> 1], (e & 1) ? 0x07060302u : 0x05040100u);
#pragma unroll
  for (int e = 0; e < 4; ++e) asm volatile("" : "+v"(BW[e]));
  PIN();
  STAMP(8);
  __syncthreads();
  {
    const v4u ao = *(const v4u*)&L.ov[(a >> 2) & 1][8 * g];
    f32x4 d[4];
#pragma unroll
    for (int e = 0; e < 4; ++e) d[e] = mfma(ao, BW[e], f32x4{0.f, 0.f, 0.f, 0.f});
    if (lane < 32) put4(rs, WS_P2 + id * 4096 + g * 2048 + wv * 256 + a * 16, tag4(d[0][0], d[1][0], d[2][0], d[3][0], tag));
  }
  PIN();
  STAMP(9);
  if (w < 64) {
    const int o0 = WS_P2 + (2 * (16 * wv + (lane >> 3)) + ((w >> 4) & 1)) * 4096 + (w >> 5) * 2048 + (w & 15) * 128 + (lane & 7) * 16;
    const int of2[2] = {o0, o0 + 16 * 4096};
    v4u yy[2] = {get4(rs, of2[0]), get4(rs, of2[1])};
    settle<2>(rs, of2, yy, tag);
    float v[4];
#pragma unroll
    for (int e = 0; e < 4; ++e) v[e] = val(yy[0][e]) + val(yy[1][e]);
    const float z = red4_hi(v);
    if ((lane & 8) == 0) L.fin[wv][4 * (lane & 7) + 2 * (lane >> 5) + ((lane >> 4) & 1)] = z;
    STAMP(10);
    __syncthreads();
    if (t < 16) {
      f32x2 sm = *(const f32x2*)&L.fin[0][2 * t];
#pragma unroll
      for (int ww = 1; ww < NWAVE; ++ww) sm += *(const f32x2*)&L.fin[ww][2 * t];
      ((unsigned*)out)[w * 16 + t] = cvtpk(sm[0], sm[1]);
    }
    if (w == 0 && t == 0) __hip_atomic_store((gu32*)(ws + WS_EPOCH), tag, RLX_AGENT);
  } else { STAMP(10); }
}

extern "C" void kernel_launch(void* const* d_in, const int* in_sizes, int n_in, void* d_out, int out_size, void* d_ws,
                              size_t ws_size, hipStream_t stream) {
  constexpr int kLds = 96 * 1024;
  (void)hipFuncSetAttribute((const void*)k_fused, hipFuncAttributeMaxDynamicSharedMemorySize, kLds);
  k_fused<<<NWG, NTHR, kLds, stream>>>((const unsigned short*)d_in[0], (const unsigned short*)d_in[1], (const unsigned short*)d_in[2],
                                       (const unsigned short*)d_in[3], (const unsigned short*)d_in[4], (unsigned short*)d_out,
                                       (unsigned char*)d_ws);
}
```

```cpp
#include <hip/hip_runtime.h>
#include <hip/hip_bf16.h>
#include <stdint.h>

typedef unsigned v4u __attribute__((ext_vector_type(4)));
typedef unsigned v2u __attribute__((ext_vector_type(2)));
typedef float f32x4 __attribute__((ext_vector_type(4)));
typedef float f32x2 __attribute__((ext_vector_type(2)));
typedef short bf16x8 __attribute__((ext_vector_type(8)));
typedef __bf16 bf16x2_t __attribute__((ext_vector_type(2)));
#define GAS __attribute__((address_space(1)))
#define LAS __attribute__((address_space(3)))
typedef GAS unsigned gu32;
typedef unsigned long long u64;
#define RLX_AGENT __ATOMIC_RELAXED, __HIP_MEMORY_SCOPE_AGENT
#define RLX_WG __ATOMIC_RELAXED, __HIP_MEMORY_SCOPE_WORKGROUP

constexpr int SK = 32768, DH = 128;
constexpr int NWG = 256, NTHR = 512, NWAVE = 8;
constexpr float C2 = 0.08838834764831845f * 1.4426950408889634f;
constexpr unsigned kSpinCap = 1u << 17;

#define PACE 2
#define PACEV PACE
#define NPRE 4
#define QPOLL_AT 10
#define WOPOS 2
#define WQSKEW 0
#define QLAG 1
#define NAP 1
#define STR_(x) #x
#define STR(x) STR_(x)
#define PACEWAIT() asm volatile("s_waitcnt vmcnt(" STR(PACE) ")" ::: "memory")
#define PACEWAITV() asm volatile("s_waitcnt vmcnt(" STR(PACEV) ")" ::: "memory")
#define PIN() do { asm volatile("" ::: "memory"); __builtin_amdgcn_sched_barrier(0); } while (0)

constexpr int WS_EPOCH = 0;
constexpr int WS_QP = 4096;
constexpr int WS_PO = WS_QP + 256 * 512;
constexpr int PO_STR = 4096 + 128;
constexpr int WS_P2 = WS_PO + 256 * PO_STR;
constexpr int WS_END = WS_P2 + 128 * 8192;
#define STAMP(k) do {} while (0)

__device__ __forceinline__ v4u ldnt(const void* p) { return __builtin_nontemporal_load((const GAS v4u*)p); }
__device__ __forceinline__ v2u ldnt64(const void* p) { return __builtin_nontemporal_load((const GAS v2u*)p); }
__device__ __forceinline__ f32x4 mfma(v4u a, v4u b, f32x4 c) {
  return __builtin_amdgcn_mfma_f32_16x16x32_bf16(__builtin_bit_cast(bf16x8, a), __builtin_bit_cast(bf16x8, b), c, 0, 0, 0);
}
__device__ __forceinline__ unsigned fbits(float f) { return __builtin_bit_cast(unsigned, f); }
__device__ __forceinline__ float bitsf(unsigned u) { return __builtin_bit_cast(float, u); }
__device__ __forceinline__ float bflo(unsigned u) { return bitsf(u << 16); }
__device__ __forceinline__ float bfhi(unsigned u) { return bitsf(u & 0xffff0000u); }
__device__ __forceinline__ unsigned cvtpk(float lo, float hi) {
  f32x2 v = {lo, hi};
  return __builtin_bit_cast(unsigned, __builtin_convertvector(v, bf16x2_t));
}
template <int CTRL> __device__ __forceinline__ float dpp(float x) {
  return bitsf(__builtin_amdgcn_mov_dpp((int)fbits(x), CTRL, 0xf, 0xf, true));
}
constexpr int ROR8 = 0x128;
__device__ __forceinline__ float swap32_add(float x, float y) {
  auto r = __builtin_amdgcn_permlane32_swap(fbits(x), fbits(y), false, false);
  return bitsf(r[0]) + bitsf(r[1]);
}
__device__ __forceinline__ float swap16_add(float x, float y) {
  auto r = __builtin_amdgcn_permlane16_swap(fbits(x), fbits(y), false, false);
  return bitsf(r[0]) + bitsf(r[1]);
}
__device__ __forceinline__ float swap32_max(float x) {
  auto r = __builtin_amdgcn_permlane32_swap(fbits(x), fbits(x), false, false);
  return fmaxf(bitsf(r[0]), bitsf(r[1]));
}
__device__ __forceinline__ float swap16_max(float x) {
  auto r = __builtin_amdgcn_permlane16_swap(fbits(x), fbits(x), false, false);
  return fmaxf(bitsf(r[0]), bitsf(r[1]));
}
__device__ __forceinline__ float red4_hi(const float (&v)[4]) {
  const float u0 = swap32_add(v[0], v[2]), u1 = swap32_add(v[1], v[3]);
  const float z = swap16_add(u0, u1);
  return z + dpp<ROR8>(z);
}
__device__ __forceinline__ v4u tag4(float a, float b, float c, float d, unsigned tag) {
  return v4u{__builtin_amdgcn_perm(fbits(a), tag, 0x07060500u), __builtin_amdgcn_perm(fbits(b), tag, 0x07060501u),
             __builtin_amdgcn_perm(fbits(c), tag, 0x07060502u), __builtin_amdgcn_perm(fbits(d), tag, 0x07060503u)};
}
__device__ __forceinline__ bool ok4(v4u x, unsigned tag) {
  const unsigned lo = __builtin_amdgcn_perm(x[1], x[0], 0x0c0c0400u), hi = __builtin_amdgcn_perm(x[3], x[2], 0x04000c0cu);
  return (lo | hi) == tag;
}
__device__ __forceinline__ float val(unsigned x) { return bitsf(x & 0xffffff00u); }
__device__ __forceinline__ void put4(__amdgpu_buffer_rsrc_t rs, int off, v4u v) { __builtin_amdgcn_raw_buffer_store_b128(v, rs, off, 0, 16); }
__device__ __forceinline__ v4u get4(__amdgpu_buffer_rsrc_t rs, int off) { return __builtin_amdgcn_raw_buffer_load_b128(rs, off, 0, 16); }
template <int N> __device__ __forceinline__ void settle(__amdgpu_buffer_rsrc_t rs, const int (&off)[N], v4u (&x)[N], unsigned tag) {
  bool ok = true;
#pragma unroll
  for (int k = 0; k < N; ++k) ok &= ok4(x[k], tag);
  if (__builtin_expect(!__all(ok), 0)) {
#pragma unroll 1
    for (unsigned spins = 0; spins < kSpinCap; ++spins) {
      __builtin_amdgcn_s_sleep(NAP);
      asm volatile("" ::: "memory");
      if (!ok) {
#pragma unroll
        for (int k = 0; k < N; ++k) x[k] = get4(rs, off[k]);
        ok = true;
#pragma unroll
        for (int k = 0; k < N; ++k) ok &= ok4(x[k], tag);
      }
      if (__all(ok)) break;
    }
  }
}
__device__ __forceinline__ bool last_wave(unsigned* cnt, int lane) {
  unsigned old = 0;
  asm volatile("s_waitcnt lgkmcnt(0)" ::: "memory");
  if (lane == 0) old = __hip_atomic_fetch_add((LAS unsigned*)cnt, 1u, RLX_WG);
  const bool last = __builtin_amdgcn_readfirstlane(old) == NWAVE - 1;
  asm volatile("" ::: "memory");
  return last;
}

struct Lds {
  float red[NWAVE][8][128];
  float qred[NWAVE][128];
  unsigned qs[512];
  float rml[NWAVE][8][2];
  float fin[NWAVE][32];
  float mw[8][NWAVE];
  unsigned short ov[2][32];
  unsigned qready, qcnt, mlcnt;
};

__global__ void __launch_bounds__(NTHR, 2)
k_fused(const unsigned short* __restrict__ x, const unsigned short* __restrict__ kc, const unsigned short* __restrict__ vc,
        const unsigned short* __restrict__ wq, const unsigned short* __restrict__ wo, unsigned short* __restrict__ out, unsigned char* ws) {
  extern __shared__ __attribute__((aligned(16))) unsigned char smem[];
  Lds& L = *(Lds*)smem;
  STAMP(0);
  const int w = blockIdx.x, t = threadIdx.x, lane = t & 63, wv = __builtin_amdgcn_readfirstlane(t >> 6);
  const int p = w & 7, s = w >> 3, b = p >> 2, kvh = p & 3;
  const int cgq = w >> 2, kq = w & 3;
  const bool oproj = !(w & 1);
  const int rb = w >> 1;
  const int a = lane & 15, g = lane >> 4;
  const __amdgpu_buffer_rsrc_t rs = __builtin_amdgcn_make_buffer_rsrc((void*)ws, 0, WS_END, 0x00020000);

  int vzero;
  asm volatile("v_mov_b32 %0, 0" : "=v"(vzero));
  const unsigned epoch = __builtin_amdgcn_raw_buffer_load_b32(rs, WS_EPOCH + vzero, 0, 16);
  constexpr int NQR = 4;
  const int r0 = 256 * kq + 32 * wv + 4 * (lane >> 3);
  const v2u xa = *(const GAS v2u*)(x + r0), xb = *(const GAS v2u*)(x + 1024 + r0);
  v4u W[4];
#pragma unroll
  for (int i = 0; i < 4; ++i) W[i] = ldnt(wq + (size_t)(r0 + i) * 4096 + 64 * cgq + 8 * (lane & 7));
  if (t == 0) { L.qready = 0u; L.qcnt = 0u; L.mlcnt = 0u; }
  PIN();
  __syncthreads();
  PIN();
  const size_t base = ((size_t)p * SK + (size_t)s * 1024) * (DH * 2);
  const int kap = a >> 1;
  const unsigned char* kb = (const unsigned char*)kc + base + (size_t)wv * 8192 + (4 * (kap & 1) + (kap >> 1)) * 256 + (2 * g + (a & 1)) * 16;
  const unsigned char* vb = (const unsigned char*)vc + base + (size_t)wv * 8192 + lane * 16;
  v4u R[4][8];
#define ISSUE_K(n) do { R[(n) >> 2][2 * ((n) & 3)] = ldnt(kb + ((n) >> 2) * 65536 + ((n) & 3) * 2048); \
    R[(n) >> 2][2 * ((n) & 3) + 1] = ldnt(kb + ((n) >> 2) * 65536 + ((n) & 3) * 2048 + 128); PIN(); } while (0)
#define ISSUE_V(n) do { R[(n) >> 2][2 * ((n) & 3)] = ldnt(vb + ((n) >> 2) * 65536 + ((n) & 3) * 2048); \
    R[(n) >> 2][2 * ((n) & 3) + 1] = ldnt(vb + ((n) >> 2) * 65536 + ((n) & 3) * 2048 + 1024); PIN(); } while (0)
#pragma unroll
  for (int n = 0; n < NPRE; ++n) ISSUE_K(n);

  {
    float acc[2][8];
#pragma unroll
    for (int j = 0; j < 8; ++j) { acc[0][j] = 0.f; acc[1][j] = 0.f; }
#pragma unroll
    for (int i = 0; i < NQR; ++i) {
      const float x0 = (i & 1) ? bfhi(xa[i >> 1]) : bflo(xa[i >> 1]);
      const float x1 = (i & 1) ? bfhi(xb[i >> 1]) : bflo(xb[i >> 1]);
#pragma unroll
      for (int u = 0; u < 4; ++u) {
        const float wa = bflo(W[i][u]), wb = bfhi(W[i][u]);
        acc[0][2 * u] += x0 * wa; acc[0][2 * u + 1] += x0 * wb;
        acc[1][2 * u] += x1 * wa; acc[1][2 * u + 1] += x1 * wb;
      }
    }
    float v8[8], v4[4], v2[2];
#pragma unroll
    for (int j = 0; j < 8; ++j) v8[j] = swap32_add(acc[0][j], acc[1][j]);
#pragma unroll
    for (int j = 0; j < 4; ++j) v4[j] = swap16_add(v8[j], v8[j + 4]);
    const bool b3 = lane & 8;
#pragma unroll
    for (int j = 0; j < 2; ++j) {
      const float keep = b3 ? v4[j + 2] : v4[j], send = b3 ? v4[j] : v4[j + 2];
      v2[j] = keep + dpp<ROR8>(send);
    }
    const int col = 8 * (lane & 7) + 4 * ((lane >> 4) & 1) + 2 * ((lane >> 3) & 1);
    *(f32x2*)&L.qred[wv][(lane >> 5) * 64 + col] = f32x2{v2[0], v2[1]};
  }
  STAMP(1);
  const unsigned tag = ((((epoch & 0x00ff00ffu) * 5u + 0x00350047u) & 0x00ff00ffu) | ((((epoch >> 8) & 0x00ff00ffu) * 5u + 0x00a9001du) & 0x00ff00ffu) << 8);
  if (last_wave(&L.qcnt, lane)) {
    if (lane < 32) {
      f32x4 sum = *(const f32x4*)&L.qred[0][4 * lane];
#pragma unroll
      for (int ww = 1; ww < NWAVE; ++ww) sum += *(const f32x4*)&L.qred[ww][4 * lane];
      put4(rs, WS_QP + w * 512 + lane * 16, tag4(sum[0], sum[1], sum[2], sum[3], tag));
    }
  }
  PIN();
  STAMP(2);
  const int oq = WS_QP + ((16 * kvh + 2 * wv + (lane >> 5)) * 4 + 2 * (g & 1)) * 512 + b * 256 + a * 16;
  const int offq[2] = {oq, oq + 512};
  v4u xq[2];
#pragma unroll
  for (int n = NPRE; n < 16; ++n) {
    PACEWAIT(); ISSUE_K(n);
    if (n == QPOLL_AT) { xq[0] = get4(rs, offq[0]); xq[1] = get4(rs, offq[1]); PIN(); }
    if (n == QPOLL_AT + QLAG) {
      settle<2>(rs, offq, xq, tag);
      float q4[4];
#pragma unroll
      for (int e = 0; e < 4; ++e) { const float v = val(xq[0][e]) + val(xq[1][e]); q4[e] = swap16_add(v, v); }
      if (!(g & 1)) *(v2u*)&L.qs[wv * 64 + (lane >> 5) * 32 + 2 * a] = v2u{cvtpk(q4[0], q4[1]), cvtpk(q4[2], q4[3])};
      asm volatile("s_waitcnt lgkmcnt(0)" ::: "memory");
      if (lane == 0) __hip_atomic_fetch_add((LAS unsigned*)&L.qready, 1u, RLX_WG);
      PIN();
      STAMP(3);
    }
  }
  while (__hip_atomic_load((LAS unsigned*)&L.qready, RLX_WG) != NWAVE) __builtin_amdgcn_s_sleep(1);
  PIN();
  STAMP(4);
  v4u qf[2];
#pragma unroll
  for (int dh = 0; dh < 2; ++dh) qf[dh] = *(const v4u*)&L.qs[(a & 7) * 64 + 32 * dh + 8 * g + 4 * (a >> 3)];

  float S[4][8];
  const bool cp = a & 8;
#pragma unroll
  for (int i = 0; i < 4; ++i) {
#pragma unroll
    for (int gm = 0; gm < 4; ++gm) {
      f32x4 c = {0.f, 0.f, 0.f, 0.f};
      c = mfma(R[i][2 * gm], qf[0], c);
      c = mfma(R[i][2 * gm + 1], qf[1], c);
      const float u0 = cp ? c[1] : c[0], u1 = cp ? c[3] : c[2];
      S[i][2 * gm] = u0 + dpp<ROR8>(u0);
      S[i][2 * gm + 1] = u1 + dpp<ROR8>(u1);
    }
    PIN();
    if (i == 0) { ISSUE_V(0); ISSUE_V(1); }
    if (i == 3) { ISSUE_V(2); ISSUE_V(3); }
  }
  STAMP(5);
  float m = S[0][0];
#pragma unroll
  for (int i = 0; i < 4; ++i)
#pragma unroll
    for (int j = 0; j < 8; ++j) m = fmaxf(m, S[i][j]);
  m = swap16_max(m);
  m = swap32_max(m);
  const float mc = m * C2;
  float l = 0.f;
  v4u P[4];
#pragma unroll
  for (int i = 0; i < 4; ++i) {
    float e[8];
#pragma unroll
    for (int j = 0; j < 8; ++j) { e[j] = __builtin_amdgcn_exp2f(S[i][j] * C2 - mc); l += e[j]; }
    P[i] = v4u{cvtpk(e[0], e[1]), cvtpk(e[2], e[3]), cvtpk(e[4], e[5]), cvtpk(e[6], e[7])};
  }
  l = swap16_add(l, l);
  l = swap32_add(l, l);
  if (lane < 8) { L.rml[wv][lane][0] = mc; L.rml[wv][lane][1] = l; }
  if (last_wave(&L.mlcnt, lane)) {
    if (lane < 8) {
      float M = L.rml[0][lane][0];
#pragma unroll
      for (int ww = 1; ww < NWAVE; ++ww) M = fmaxf(M, L.rml[ww][lane][0]);
      M = val(fbits(M));
      float Ls = 0.f;
#pragma unroll
      for (int ww = 0; ww < NWAVE; ++ww) {
        const float f = __builtin_amdgcn_exp2f(L.rml[ww][lane][0] - M);
        L.mw[lane][ww] = f;
        Ls += f * L.rml[ww][lane][1];
      }
      put4(rs, WS_PO + w * PO_STR + 4096 + lane * 16, tag4(M, Ls, M, Ls, tag));
    }
  }
  PIN();
  f32x4 O[8];
#pragma unroll
  for (int e = 0; e < 8; ++e) O[e] = f32x4{0.f, 0.f, 0.f, 0.f};
  v4u WO[8];
  const bool comb = oproj && wv < 2;
  const int po0 = WS_PO + ((lane >> 3) * 8 + (wv & 1) * 4 + (rb >> 5)) * PO_STR;
  const int om = po0 + 4096 + ((rb >> 2) & 7) * 16;
  const int offm[4] = {om, om + 64 * PO_STR, om + 128 * PO_STR, om + 192 * PO_STR};
  v4u ML[4] = {};
#pragma unroll
  for (int i = 0; i < 4; ++i) {
    if (i == 0) { PACEWAITV(); ISSUE_V(4); PACEWAITV(); ISSUE_V(5); PACEWAITV(); ISSUE_V(6); }
    if (i == 1) { PACEWAITV(); ISSUE_V(7); PACEWAITV(); ISSUE_V(8); PACEWAITV(); ISSUE_V(9); PACEWAITV(); ISSUE_V(10); }
    if (i == 2) {
      PACEWAITV(); ISSUE_V(11); PACEWAITV(); ISSUE_V(12);
      if (comb) {
#pragma unroll
        for (int k = 0; k < 4; ++k) ML[k] = get4(rs, offm[k]);
      }
      PIN();
      PACEWAITV(); ISSUE_V(13); PACEWAITV(); ISSUE_V(14); PACEWAITV(); ISSUE_V(15);
    }
#pragma unroll
    for (int e = 0; e < 8; ++e) {
      const unsigned sel = (e & 1) ? 0x07060302u : 0x05040100u;
      v4u bv;
#pragma unroll
      for (int u = 0; u < 4; ++u) bv[u] = __builtin_amdgcn_perm(R[i][2 * u + 1][e >> 1], R[i][2 * u][e >> 1], sel);
      O[e] = mfma(P[i], bv, O[e]);
    }
    PIN();
  }
  STAMP(6);
  if (g < 2) {
#pragma unroll
    for (int r = 0; r < 4; ++r) {
      *(f32x4*)&L.red[wv][4 * g + r][8 * a] = f32x4{O[0][r], O[1][r], O[2][r], O[3][r]};
      *(f32x4*)&L.red[wv][4 * g + r][8 * a + 4] = f32x4{O[4][r], O[5][r], O[6][r], O[7][r]};
    }
  }
  __syncthreads();
  if (lane < 32) {
    const int h = wv;
    const f32x4 f0 = *(const f32x4*)&L.mw[h][0], f1 = *(const f32x4*)&L.mw[h][4];
    f32x4 o = f0[0] * *(const f32x4*)&L.red[0][h][4 * lane];
#pragma unroll
    for (int ww = 1; ww < NWAVE; ++ww) o += (ww < 4 ? f0[ww & 3] : f1[ww & 3]) * *(const f32x4*)&L.red[ww][h][4 * lane];
    put4(rs, WS_PO + w * PO_STR + h * 512 + lane * 16, tag4(o[0], o[1], o[2], o[3], tag));
  }
  PIN();
  STAMP(7);
  STAMP(8); STAMP(12); STAMP(13);
  if (!oproj) goto fin;
  {
  const int oo = po0 + ((rb >> 2) & 7) * 512 + (rb & 3) * 128 + (lane & 7) * 16;
  const int off[4] = {oo, oo + 64 * PO_STR, oo + 128 * PO_STR, oo + 192 * PO_STR};
  v4u xx[4] = {};
  if (wv < 2) {
#pragma unroll
    for (int k = 0; k < 4; ++k) xx[k] = get4(rs, off[k]);
  }
  PIN();
  if (oproj) {
#pragma unroll
    for (int j = 0; j < 8; ++j) WO[j] = ldnt(wo + (size_t)(32 * rb + 8 * g + j) * 1024 + 128 * wv + 8 * a);
  }
  PIN();
  if (wv < 2) {
    float wt[4];
    settle<4>(rs, offm, ML, tag);
    float M = fmaxf(fmaxf(val(ML[0][0]), val(ML[1][0])), fmaxf(val(ML[2][0]), val(ML[3][0])));
    M = fmaxf(M, dpp<ROR8>(M));
    M = swap16_max(M); M = swap32_max(M);
    float Ls = 0.f;
#pragma unroll
    for (int k = 0; k < 4; ++k) { wt[k] = __builtin_amdgcn_exp2f(val(ML[k][0]) - M); Ls += wt[k] * val(ML[k][1]); }
    Ls = swap32_add(Ls, Ls); Ls = swap16_add(Ls, Ls); Ls += dpp<ROR8>(Ls);
    const float inv = 1.0f / Ls;
#pragma unroll
    for (int k = 0; k < 4; ++k) { wt[k] *= inv; asm volatile("" : "+v"(wt[k])); }
    PIN();
    STAMP(12);
    settle<4>(rs, off, xx, tag);
    STAMP(13);
    float o[4];
#pragma unroll
    for (int e = 0; e < 4; ++e) o[e] = (wt[0] * val(xx[0][e]) + wt[1] * val(xx[1][e])) + (wt[2] * val(xx[2][e]) + wt[3] * val(xx[3][e]));
    const unsigned pk = cvtpk(red4_hi(o), 0.f);
    if ((lane & 8) == 0) L.ov[wv][4 * (lane & 7) + 2 * (lane >> 5) + ((lane >> 4) & 1)] = (unsigned short)pk;
  }
  PIN();
  v4u BW[8];
#pragma unroll
  for (int e = 0; e < 8; ++e)
#pragma unroll
    for (int u = 0; u < 4; ++u) BW[e][u] = __builtin_amdgcn_perm(WO[2 * u + 1][e >> 1], WO[2 * u][e >> 1], (e & 1) ? 0x07060302u : 0x05040100u);
#pragma unroll
  for (int e = 0; e < 8; ++e) asm volatile("" : "+v"(BW[e]));
  PIN();
  STAMP(8);
  __syncthreads();
  {
    const v4u ao = *(const v4u*)&L.ov[(a >> 2) & 1][8 * g];
    f32x4 d[8];
#pragma unroll
    for (int e = 0; e < 8; ++e) d[e] = mfma(ao, BW[e], f32x4{0.f, 0.f, 0.f, 0.f});
    const bool hi = g >> 1;
    put4(rs, WS_P2 + rb * 8192 + (g & 1) * 4096 + wv * 512 + a * 32 + (g >> 1) * 16,
         tag4(hi ? d[4][0] : d[0][0], hi ? d[5][0] : d[1][0], hi ? d[6][0] : d[2][0], hi ? d[7][0] : d[3][0], tag));
  }
  }
fin:
  PIN();
  STAMP(9); STAMP(14); STAMP(15);
  if (!oproj && w < 128) {
    const int o0 = WS_P2 + (16 * wv + (lane >> 3)) * 8192 + rb * 128 + (lane & 7) * 16;
    const int of2[2] = {o0, o0 + 8 * 8192};
    v4u yy[2] = {get4(rs, of2[0]), get4(rs, of2[1])};
    settle<2>(rs, of2, yy, tag);
    STAMP(14);
    float v[4];
#pragma unroll
    for (int e = 0; e < 4; ++e) v[e] = val(yy[0][e]) + val(yy[1][e]);
    const float z = red4_hi(v);
    if ((lane & 8) == 0) L.fin[wv][4 * (lane & 7) + 2 * (lane >> 5) + ((lane >> 4) & 1)] = z;
    STAMP(10);
    __syncthreads();
    STAMP(15);
    if (t < 16) {
      f32x2 sm = *(const f32x2*)&L.fin[0][2 * t];
#pragma unroll
      for (int ww = 1; ww < NWAVE; ++ww) sm += *(const f32x2*)&L.fin[ww][2 * t];
      ((unsigned*)out)[rb * 16 + t] = cvtpk(sm[0], sm[1]);
    }
    if (w == 1 && t == 0) __hip_atomic_store((gu32*)(ws + WS_EPOCH), tag, RLX_AGENT);
  } else { STAMP(10); }
}

extern "C" void kernel_launch(void* const* d_in, const int* in_sizes, int n_in, void* d_out, int out_size, void* d_ws,
                              size_t ws_size, hipStream_t stream) {
  constexpr int kLds = 96 * 1024;
  (void)hipFuncSetAttribute((const void*)k_fused, hipFuncAttributeMaxDynamicSharedMemorySize, kLds);
  k_fused<<<NWG, NTHR, kLds, stream>>>((const unsigned short*)d_in[0], (const unsigned short*)d_in[1], (const unsigned short*)d_in[2],
                                       (const unsigned short*)d_in[3], (const unsigned short*)d_in[4], (unsigned short*)d_out,
                                       (unsigned char*)d_ws);
}
```

```cpp
#include <hip/hip_runtime.h>
#include <hip/hip_bf16.h>
#include <stdint.h>

typedef unsigned v4u __attribute__((ext_vector_type(4)));
typedef unsigned v2u __attribute__((ext_vector_type(2)));
typedef float f32x4 __attribute__((ext_vector_type(4)));
typedef float f32x2 __attribute__((ext_vector_type(2)));
typedef short bf16x8 __attribute__((ext_vector_type(8)));
typedef __bf16 bf16x2_t __attribute__((ext_vector_type(2)));
typedef unsigned long long u64;
#define GAS __attribute__((address_space(1)))
#define LAS __attribute__((address_space(3)))
#define RLX_WG __ATOMIC_RELAXED, __HIP_MEMORY_SCOPE_WORKGROUP

constexpr int SK = 32768, DH = 128;
constexpr int NWG = 256, NTHR = 512, NWAVE = 8;
constexpr float C2 = 0.08838834764831845f * 1.4426950408889634f;
constexpr unsigned kSpinCap = 1u << 17;
constexpr int NPRE = 4;
constexpr int QPOLL_AT = 10;
constexpr int ECB = 6;
constexpr int AUX_SC1 = 16;

constexpr int T0 = 180, TAU = 61;
#define PACE(n) do { asm volatile("s_waitcnt vmcnt(2)" ::: "memory"); \
    while ((int)((unsigned)__builtin_amdgcn_s_memrealtime() - (tb + (unsigned)(((n) - 4) * TAU))) < 0) __builtin_amdgcn_s_sleep(1); } while (0)
#define PIN() do { asm volatile("" ::: "memory"); __builtin_amdgcn_sched_barrier(0); } while (0)

constexpr int WS_EPOCH = 0;
constexpr int WS_QP = 4096;
constexpr int WS_PO = WS_QP + 256 * 512;
constexpr int PO_STR = 4096 + 128;
constexpr int WS_P2 = WS_PO + 256 * PO_STR;
constexpr int WS_END = WS_P2 + 128 * 8192;
#define STAMP(k) do {} while (0)

__device__ __forceinline__ v4u ldnt(const void* p) { return __builtin_nontemporal_load((const GAS v4u*)p); }
__device__ __forceinline__ f32x4 mfma(v4u a, v4u b, f32x4 c) {
  return __builtin_amdgcn_mfma_f32_16x16x32_bf16(__builtin_bit_cast(bf16x8, a), __builtin_bit_cast(bf16x8, b), c, 0, 0, 0);
}
__device__ __forceinline__ unsigned fbits(float f) { return __builtin_bit_cast(unsigned, f); }
__device__ __forceinline__ float bitsf(unsigned u) { return __builtin_bit_cast(float, u); }
__device__ __forceinline__ float bflo(unsigned u) { return bitsf(u << 16); }
__device__ __forceinline__ float bfhi(unsigned u) { return bitsf(u & 0xffff0000u); }
__device__ __forceinline__ unsigned cvtpk(float lo, float hi) {
  f32x2 v = {lo, hi};
  return __builtin_bit_cast(unsigned, __builtin_convertvector(v, bf16x2_t));
}
__device__ __forceinline__ float ror8(float x) {
  return bitsf(__builtin_amdgcn_mov_dpp((int)fbits(x), 0x128, 0xf, 0xf, true));
}
__device__ __forceinline__ float swap32_add(float x, float y) {
  auto r = __builtin_amdgcn_permlane32_swap(fbits(x), fbits(y), false, false);
  return bitsf(r[0]) + bitsf(r[1]);
}
__device__ __forceinline__ float swap16_add(float x, float y) {
  auto r = __builtin_amdgcn_permlane16_swap(fbits(x), fbits(y), false, false);
  return bitsf(r[0]) + bitsf(r[1]);
}
__device__ __forceinline__ float swap32_max(float x) {
  auto r = __builtin_amdgcn_permlane32_swap(fbits(x), fbits(x), false, false);
  return fmaxf(bitsf(r[0]), bitsf(r[1]));
}
__device__ __forceinline__ float swap16_max(float x) {
  auto r = __builtin_amdgcn_permlane16_swap(fbits(x), fbits(x), false, false);
  return fmaxf(bitsf(r[0]), bitsf(r[1]));
}
__device__ __forceinline__ float red4_hi(const float (&v)[4]) {
  const float u0 = swap32_add(v[0], v[2]), u1 = swap32_add(v[1], v[3]);
  const float z = swap16_add(u0, u1);
  return z + ror8(z);
}

constexpr unsigned TM = 0x3ffu;
__device__ __forceinline__ v4u tag4u(unsigned a, unsigned b, unsigned c, unsigned d, v4u tg) {
  return v4u{(a & ~TM) | tg[0], (b & ~TM) | tg[1], (c & ~TM) | tg[2], (d & ~TM) | tg[3]};
}
__device__ __forceinline__ v4u tag4(float a, float b, float c, float d, v4u tg) { return tag4u(fbits(a), fbits(b), fbits(c), fbits(d), tg); }
__device__ __forceinline__ bool ok4(v4u x, v4u tg) { return ((((x[0] ^ tg[0]) | (x[1] ^ tg[1])) | ((x[2] ^ tg[2]) | (x[3] ^ tg[3]))) & TM) == 0u; }
__device__ __forceinline__ float val(unsigned x) { return bitsf(x & ~TM); }
__device__ __forceinline__ float val2(unsigned hi, unsigned lo) { return bitsf((hi & ~TM) | ((lo >> 10) & TM)); }
__device__ __forceinline__ void put4(__amdgpu_buffer_rsrc_t rs, int off, v4u v) { __builtin_amdgcn_raw_buffer_store_b128(v, rs, off, 0, AUX_SC1); }
__device__ __forceinline__ v4u get4(__amdgpu_buffer_rsrc_t rs, int off) { return __builtin_amdgcn_raw_buffer_load_b128(rs, off, 0, AUX_SC1); }
template <int N> __device__ __forceinline__ void settle(__amdgpu_buffer_rsrc_t rs, const int (&off)[N], v4u (&x)[N], v4u tg) {
  bool ok = true;
#pragma unroll
  for (int k = 0; k < N; ++k) ok &= ok4(x[k], tg);
  if (__builtin_expect(!__all(ok), 0)) {
#pragma unroll 1
    for (unsigned spins = 0; spins < kSpinCap; ++spins) {
      __builtin_amdgcn_s_sleep(1);
      asm volatile("" ::: "memory");
      if (!ok) {
#pragma unroll
        for (int k = 0; k < N; ++k) x[k] = get4(rs, off[k]);
        ok = true;
#pragma unroll
        for (int k = 0; k < N; ++k) ok &= ok4(x[k], tg);
      }
      if (__all(ok)) break;
    }
  }
}
__device__ __forceinline__ bool last_wave(unsigned* cnt, int lane) {
  unsigned old = 0;
  asm volatile("s_waitcnt lgkmcnt(0)" ::: "memory");
  if (lane == 0) old = __hip_atomic_fetch_add((LAS unsigned*)cnt, 1u, RLX_WG);
  const bool last = __builtin_amdgcn_readfirstlane(old) == NWAVE - 1;
  asm volatile("" ::: "memory");
  return last;
}

struct Lds {
  float red[NWAVE][8][128];
  float qred[NWAVE][128];
  unsigned qs[512];
  float rml[NWAVE][8][2];
  float fin[NWAVE][32];
  float mw[8][NWAVE];
  unsigned short ov[2][32];
  unsigned qready, qcnt, mlcnt;
};

__global__ void __launch_bounds__(NTHR, 2)
gqa_decode_fused(const unsigned short* __restrict__ x, const unsigned short* __restrict__ kc, const unsigned short* __restrict__ vc,
                 const unsigned short* __restrict__ wq, const unsigned short* __restrict__ wo, unsigned short* __restrict__ out,
                 unsigned char* ws) {
  extern __shared__ __attribute__((aligned(16))) unsigned char smem[];
  Lds& L = *(Lds*)smem;
  STAMP(0);
  const int w = blockIdx.x, t = threadIdx.x, lane = t & 63, wv = __builtin_amdgcn_readfirstlane(t >> 6);
  const unsigned tb = (unsigned)__builtin_amdgcn_s_memrealtime() + T0 + wv * (TAU / 8);
  const int p = w & 7, s = w >> 3, b = p >> 2, kvh = p & 3;
  const int cgq = w >> 2, kq = w & 3;
  const int rb = w >> 1;
  const int cb = (w & 1) ? ECB + wv - 2 : wv - 2;
  const bool oproj = wv >= 2 && cb < ((w & 1) ? 8 : ECB), comb = wv < 2;
  const int a = lane & 15, g = lane >> 4;
  const __amdgpu_buffer_rsrc_t rs = __builtin_amdgcn_make_buffer_rsrc((void*)ws, 0, WS_END, 0x00020000);

  int vzero;
  asm volatile("v_mov_b32 %0, 0" : "=v"(vzero));
  const v2u epoch = __builtin_amdgcn_raw_buffer_load_b64(rs, WS_EPOCH + vzero, 0, AUX_SC1);
  const int r0 = 256 * kq + 32 * wv + 4 * (lane >> 3);
  const v2u xa = *(const GAS v2u*)(x + r0), xb = *(const GAS v2u*)(x + 1024 + r0);
  v4u W[4];
#pragma unroll
  for (int i = 0; i < 4; ++i) W[i] = ldnt(wq + (size_t)(r0 + i) * 4096 + 64 * cgq + 8 * (lane & 7));
  if (t == 0) { L.qready = 0u; L.qcnt = 0u; L.mlcnt = 0u; }
  PIN();
  __syncthreads();
  PIN();
  const size_t base = ((size_t)p * SK + (size_t)s * 1024 + wv * 16) * (DH * 2);
  const int kap = a >> 1;
  const unsigned char* kb = (const unsigned char*)kc + base + (4 * (kap & 1) + (kap >> 1)) * 256 + (2 * g + (a & 1)) * 16;
  const unsigned char* vb = (const unsigned char*)vc + base + lane * 16;
  v4u R[4][8];
#define GOFF(n) (((n) >> 1) * 32768 + ((n) & 1) * 2048)
#define ISSUE_K(n) do { R[(n) >> 2][2 * ((n) & 3)] = ldnt(kb + GOFF(n)); R[(n) >> 2][2 * ((n) & 3) + 1] = ldnt(kb + GOFF(n) + 128); PIN(); } while (0)
#define ISSUE_V(n) do { R[(n) >> 2][2 * ((n) & 3)] = ldnt(vb + GOFF(n)); R[(n) >> 2][2 * ((n) & 3) + 1] = ldnt(vb + GOFF(n) + 1024); PIN(); } while (0)
#pragma unroll
  for (int n = 0; n < NPRE; ++n) ISSUE_K(n);

  {
    float acc[2][8];
#pragma unroll
    for (int j = 0; j < 8; ++j) { acc[0][j] = 0.f; acc[1][j] = 0.f; }
#pragma unroll
    for (int i = 0; i < 4; ++i) {
      const float x0 = (i & 1) ? bfhi(xa[i >> 1]) : bflo(xa[i >> 1]);
      const float x1 = (i & 1) ? bfhi(xb[i >> 1]) : bflo(xb[i >> 1]);
#pragma unroll
      for (int u = 0; u < 4; ++u) {
        const float wa = bflo(W[i][u]), wb = bfhi(W[i][u]);
        acc[0][2 * u] += x0 * wa; acc[0][2 * u + 1] += x0 * wb;
        acc[1][2 * u] += x1 * wa; acc[1][2 * u + 1] += x1 * wb;
      }
    }
    float v8[8], v4[4], v2[2];
#pragma unroll
    for (int j = 0; j < 8; ++j) v8[j] = swap32_add(acc[0][j], acc[1][j]);
#pragma unroll
    for (int j = 0; j < 4; ++j) v4[j] = swap16_add(v8[j], v8[j + 4]);
    const bool b3 = lane & 8;
#pragma unroll
    for (int j = 0; j < 2; ++j) {
      const float keep = b3 ? v4[j + 2] : v4[j], send = b3 ? v4[j] : v4[j + 2];
      v2[j] = keep + ror8(send);
    }
    const int col = 8 * (lane & 7) + 4 * ((lane >> 4) & 1) + 2 * ((lane >> 3) & 1);
    *(f32x2*)&L.qred[wv][(lane >> 5) * 64 + col] = f32x2{v2[0], v2[1]};
  }
  STAMP(1);
  const v4u tg = {((epoch[0] & TM) * 5u + 0x147u) & TM, (((epoch[0] >> 10) & TM) * 5u + 0x2b5u) & TM,
                  ((epoch[1] & TM) * 5u + 0x09du) & TM, (((epoch[1] >> 10) & TM) * 5u + 0x3a9u) & TM};
  if (last_wave(&L.qcnt, lane)) {
    if (lane < 32) {
      f32x4 sum = *(const f32x4*)&L.qred[0][4 * lane];
#pragma unroll
      for (int ww = 1; ww < NWAVE; ++ww) sum += *(const f32x4*)&L.qred[ww][4 * lane];
      put4(rs, WS_QP + w * 512 + lane * 16, tag4(sum[0], sum[1], sum[2], sum[3], tg));
    }
  }
  PIN();
  STAMP(2);
  const int oq = WS_QP + ((16 * kvh + 2 * wv + (lane >> 5)) * 4 + 2 * (g & 1)) * 512 + b * 256 + a * 16;
  const int offq[2] = {oq, oq + 512};
  v4u xq[2];
#pragma unroll
  for (int n = NPRE; n < 16; ++n) {
    PACE(n); ISSUE_K(n);
    if (n == QPOLL_AT) { xq[0] = get4(rs, offq[0]); xq[1] = get4(rs, offq[1]); PIN(); }
    if (n == QPOLL_AT + 1) {
      settle<2>(rs, offq, xq, tg);
      float q4[4];
#pragma unroll
      for (int e = 0; e < 4; ++e) { const float v = val(xq[0][e]) + val(xq[1][e]); q4[e] = swap16_add(v, v); }
      if (!(g & 1)) *(v2u*)&L.qs[wv * 64 + (lane >> 5) * 32 + 2 * a] = v2u{cvtpk(q4[0], q4[1]), cvtpk(q4[2], q4[3])};
      asm volatile("s_waitcnt lgkmcnt(0)" ::: "memory");
      if (lane == 0) __hip_atomic_fetch_add((LAS unsigned*)&L.qready, 1u, RLX_WG);
      PIN();
      STAMP(3);
    }
  }
  while (__hip_atomic_load((LAS unsigned*)&L.qready, RLX_WG) != NWAVE) __builtin_amdgcn_s_sleep(1);
  PIN();
  STAMP(4);
  v4u qf[2];
#pragma unroll
  for (int dh = 0; dh < 2; ++dh) qf[dh] = *(const v4u*)&L.qs[(a & 7) * 64 + 32 * dh + 8 * g + 4 * (a >> 3)];

  float S[4][8];
  const bool cp = a & 8;
#pragma unroll
  for (int i = 0; i < 4; ++i) {
#pragma unroll
    for (int gm = 0; gm < 4; ++gm) {
      f32x4 c = {0.f, 0.f, 0.f, 0.f};
      c = mfma(R[i][2 * gm], qf[0], c);
      c = mfma(R[i][2 * gm + 1], qf[1], c);
      const float u0 = cp ? c[1] : c[0], u1 = cp ? c[3] : c[2];
      S[i][2 * gm] = u0 + ror8(u0);
      S[i][2 * gm + 1] = u1 + ror8(u1);
    }
    PIN();
    if (i == 0) { ISSUE_V(0); ISSUE_V(1); }
    if (i == 3) { ISSUE_V(2); ISSUE_V(3); }
  }
  STAMP(5);
  float m = S[0][0];
#pragma unroll
  for (int i = 0; i < 4; ++i)
#pragma unroll
    for (int j = 0; j < 8; ++j) m = fmaxf(m, S[i][j]);
  m = swap16_max(m);
  m = swap32_max(m);
  const float mc = m * C2;
  float l = 0.f;
  v4u P[4];
#pragma unroll
  for (int i = 0; i < 4; ++i) {
    float e[8];
#pragma unroll
    for (int j = 0; j < 8; ++j) { e[j] = __builtin_amdgcn_exp2f(S[i][j] * C2 - mc); l += e[j]; }
    P[i] = v4u{cvtpk(e[0], e[1]), cvtpk(e[2], e[3]), cvtpk(e[4], e[5]), cvtpk(e[6], e[7])};
  }
  l = swap16_add(l, l);
  l = swap32_add(l, l);
  if (lane < 8) { L.rml[wv][lane][0] = mc; L.rml[wv][lane][1] = l; }
  if (last_wave(&L.mlcnt, lane)) {
    if (lane < 8) {
      float M = L.rml[0][lane][0];
#pragma unroll
      for (int ww = 1; ww < NWAVE; ++ww) M = fmaxf(M, L.rml[ww][lane][0]);
      float Ls = 0.f;
#pragma unroll
      for (int ww = 0; ww < NWAVE; ++ww) {
        const float f = __builtin_amdgcn_exp2f(L.rml[ww][lane][0] - M);
        L.mw[lane][ww] = f;
        Ls += f * L.rml[ww][lane][1];
      }
      put4(rs, WS_PO + w * PO_STR + 4096 + lane * 16, tag4u(fbits(M), fbits(Ls), fbits(M) << 10, fbits(Ls) << 10, tg));
    }
  }
  PIN();
  f32x4 O[8];
#pragma unroll
  for (int e = 0; e < 8; ++e) O[e] = f32x4{0.f, 0.f, 0.f, 0.f};
  const int po0 = WS_PO + ((lane >> 3) * 8 + (wv & 1) * 4 + (rb >> 5)) * PO_STR;
  const int om = po0 + 4096 + ((rb >> 2) & 7) * 16;
  const int offm[4] = {om, om + 64 * PO_STR, om + 128 * PO_STR, om + 192 * PO_STR};
  v4u ML[4] = {};
#pragma unroll
  for (int i = 0; i < 4; ++i) {
    if (i == 0) { PACE(16 + 4); ISSUE_V(4); PACE(16 + 5); ISSUE_V(5); PACE(16 + 6); ISSUE_V(6); }
    if (i == 1) { PACE(16 + 7); ISSUE_V(7); PACE(16 + 8); ISSUE_V(8); PACE(16 + 9); ISSUE_V(9); PACE(16 + 10); ISSUE_V(10); }
    if (i == 2) {
      PACE(16 + 11); ISSUE_V(11); PACE(16 + 12); ISSUE_V(12);
      if (comb) {
#pragma unroll
        for (int k = 0; k < 4; ++k) ML[k] = get4(rs, offm[k]);
      }
      PIN();
      PACE(16 + 13); ISSUE_V(13); PACE(16 + 14); ISSUE_V(14); PACE(16 + 15); ISSUE_V(15);
    }
#pragma unroll
    for (int e = 0; e < 8; ++e) {
      const unsigned sel = (e & 1) ? 0x07060302u : 0x05040100u;
      v4u bv;
#pragma unroll
      for (int u = 0; u < 4; ++u) bv[u] = __builtin_amdgcn_perm(R[i][2 * u + 1][e >> 1], R[i][2 * u][e >> 1], sel);
      O[e] = mfma(P[i], bv, O[e]);
    }
    PIN();
  }
  STAMP(6);
  v4u WO[8];
  if (oproj) {
#pragma unroll
    for (int j = 0; j < 8; ++j) WO[j] = ldnt(wo + (size_t)(32 * rb + 8 * g + j) * 1024 + 128 * cb + 8 * a);
  }
  PIN();
  if (g < 2) {
#pragma unroll
    for (int r = 0; r < 4; ++r) {
      *(f32x4*)&L.red[wv][4 * g + r][8 * a] = f32x4{O[0][r], O[1][r], O[2][r], O[3][r]};
      *(f32x4*)&L.red[wv][4 * g + r][8 * a + 4] = f32x4{O[4][r], O[5][r], O[6][r], O[7][r]};
    }
  }
  __syncthreads();
  if (lane < 32) {
    const f32x4 f0 = *(const f32x4*)&L.mw[wv][0], f1 = *(const f32x4*)&L.mw[wv][4];
    f32x4 pp[NWAVE];
#pragma unroll
    for (int ww = 0; ww < NWAVE; ++ww) pp[ww] = *(const f32x4*)&L.red[ww][wv][4 * lane];
    __builtin_amdgcn_sched_barrier(0);
    const f32x4 o = ((f0[0] * pp[0] + f0[1] * pp[1]) + (f0[2] * pp[2] + f0[3] * pp[3])) + ((f1[0] * pp[4] + f1[1] * pp[5]) + (f1[2] * pp[6] + f1[3] * pp[7]));
    put4(rs, WS_PO + w * PO_STR + wv * 512 + lane * 16, tag4(o[0], o[1], o[2], o[3], tg));
  }
  PIN();
  STAMP(7);
  v4u BW[8];
  if (comb) {
    const int oo = po0 + ((rb >> 2) & 7) * 512 + (rb & 3) * 128 + (lane & 7) * 16;
    const int off[4] = {oo, oo + 64 * PO_STR, oo + 128 * PO_STR, oo + 192 * PO_STR};
    v4u xx[4];
#pragma unroll
    for (int k = 0; k < 4; ++k) xx[k] = get4(rs, off[k]);
    PIN();
    float wt[4];
    settle<4>(rs, offm, ML, tg);
    float ms[4];
#pragma unroll
    for (int k = 0; k < 4; ++k) ms[k] = val2(ML[k][0], ML[k][2]);
    float M = fmaxf(fmaxf(ms[0], ms[1]), fmaxf(ms[2], ms[3]));
    M = fmaxf(M, ror8(M));
    M = swap16_max(M); M = swap32_max(M);
    float Ls = 0.f;
#pragma unroll
    for (int k = 0; k < 4; ++k) { wt[k] = __builtin_amdgcn_exp2f(ms[k] - M); Ls += wt[k] * val2(ML[k][1], ML[k][3]); }
    Ls = swap32_add(Ls, Ls); Ls = swap16_add(Ls, Ls); Ls += ror8(Ls);
    const float inv = 1.0f / Ls;
#pragma unroll
    for (int k = 0; k < 4; ++k) { wt[k] *= inv; asm volatile("" : "+v"(wt[k])); }
    PIN();
    settle<4>(rs, off, xx, tg);
    float o[4];
#pragma unroll
    for (int e = 0; e < 4; ++e) o[e] = (wt[0] * val(xx[0][e]) + wt[1] * val(xx[1][e])) + (wt[2] * val(xx[2][e]) + wt[3] * val(xx[3][e]));
    const unsigned pk = cvtpk(red4_hi(o), 0.f);
    if ((lane & 8) == 0) L.ov[wv][4 * (lane & 7) + 2 * (lane >> 5) + ((lane >> 4) & 1)] = (unsigned short)pk;
  } else if (oproj) {
#pragma unroll
    for (int e = 0; e < 8; ++e)
#pragma unroll
      for (int u = 0; u < 4; ++u) BW[e][u] = __builtin_amdgcn_perm(WO[2 * u + 1][e >> 1], WO[2 * u][e >> 1], (e & 1) ? 0x07060302u : 0x05040100u);
#pragma unroll
    for (int e = 0; e < 8; ++e) asm volatile("" : "+v"(BW[e]));
  }
  PIN();
  STAMP(8);
  __syncthreads();
  if (oproj) {
    const v4u ao = *(const v4u*)&L.ov[(a >> 2) & 1][8 * g];
    f32x4 d[8];
#pragma unroll
    for (int e = 0; e < 8; ++e) d[e] = mfma(ao, BW[e], f32x4{0.f, 0.f, 0.f, 0.f});
    const bool hi = g >> 1;
    put4(rs, WS_P2 + rb * 8192 + (g & 1) * 4096 + cb * 512 + a * 32 + (g >> 1) * 16,
         tag4(hi ? d[4][0] : d[0][0], hi ? d[5][0] : d[1][0], hi ? d[6][0] : d[2][0], hi ? d[7][0] : d[3][0], tg));
  }
  PIN();
  STAMP(9);
  if ((w & 1) && w < 128) {
    const int o0 = WS_P2 + (16 * wv + (lane >> 3)) * 8192 + rb * 128 + (lane & 7) * 16;
    const int of2[2] = {o0, o0 + 8 * 8192};
    v4u yy[2] = {get4(rs, of2[0]), get4(rs, of2[1])};
    settle<2>(rs, of2, yy, tg);
    float v[4];
#pragma unroll
    for (int e = 0; e < 4; ++e) v[e] = val(yy[0][e]) + val(yy[1][e]);
    const float z = red4_hi(v);
    if ((lane & 8) == 0) L.fin[wv][4 * (lane & 7) + 2 * (lane >> 5) + ((lane >> 4) & 1)] = z;
    STAMP(10);
    __syncthreads();
    if (t < 16) {
      f32x2 sm = *(const f32x2*)&L.fin[0][2 * t];
#pragma unroll
      for (int ww = 1; ww < NWAVE; ++ww) sm += *(const f32x2*)&L.fin[ww][2 * t];
      ((unsigned*)out)[rb * 16 + t] = cvtpk(sm[0], sm[1]);
    }
    if (w == 1 && t == 0) __builtin_amdgcn_raw_buffer_store_b64(v2u{tg[0] | (tg[1] << 10), tg[2] | (tg[3] << 10)}, rs, WS_EPOCH, 0, AUX_SC1);
  } else { STAMP(10); }
}

extern "C" void kernel_launch(void* const* d_in, const int* in_sizes, int n_in, void* d_out, int out_size, void* d_ws,
                              size_t ws_size, hipStream_t stream) {
  constexpr int kLds = 96 * 1024;
  static_assert(sizeof(Lds) <= kLds, "LDS");
  (void)hipFuncSetAttribute((const void*)gqa_decode_fused, hipFuncAttributeMaxDynamicSharedMemorySize, kLds);
  gqa_decode_fused<<<NWG, NTHR, kLds, stream>>>((const unsigned short*)d_in[0], (const unsigned short*)d_in[1],
                                                (const unsigned short*)d_in[2], (const unsigned short*)d_in[3],
                                                (const unsigned short*)d_in[4], (unsigned short*)d_out, (unsigned char*)d_ws);
}
```

```cpp
#include <hip/hip_runtime.h>
#include <hip/hip_bf16.h>
#include <stdint.h>

typedef unsigned v4u __attribute__((ext_vector_type(4)));
typedef unsigned v2u __attribute__((ext_vector_type(2)));
typedef float f32x4 __attribute__((ext_vector_type(4)));
typedef float f32x2 __attribute__((ext_vector_type(2)));
typedef short bf16x8 __attribute__((ext_vector_type(8)));
typedef __bf16 bf16x2_t __attribute__((ext_vector_type(2)));
typedef unsigned long long u64;
#define GAS __attribute__((address_space(1)))
#define LAS __attribute__((address_space(3)))
#define RLX_WG __ATOMIC_RELAXED, __HIP_MEMORY_SCOPE_WORKGROUP

constexpr int SK = 32768, DH = 128;
constexpr int NWG = 256, NTHR = 512, NWAVE = 8;
constexpr float C2 = 0.08838834764831845f * 1.4426950408889634f;
constexpr unsigned kSpinCap = 1u << 17;
constexpr int NPRE = 4;
constexpr int QPOLL_AT = 10;
constexpr int ECB = 6;
constexpr int AUX_SC1 = 16;

constexpr int T0 = 30, TAU = 67;
#define PACE(n) do { asm volatile("s_waitcnt vmcnt(2)" ::: "memory"); \
    while ((int)((unsigned)__builtin_amdgcn_s_memrealtime() - (tb + (unsigned)(((n) - 4) * TAU))) < 0) __builtin_amdgcn_s_sleep(1); } while (0)
constexpr int NAP1 = 26, NAP2 = 24;
#define PIN() do { asm volatile("" ::: "memory"); __builtin_amdgcn_sched_barrier(0); } while (0)

constexpr int WS_EPOCH = 0;
constexpr int WS_QP = 4096;
constexpr int WS_PO = WS_QP + 256 * 512;
constexpr int PO_STR = 4096 + 128;
constexpr int WS_P2 = WS_PO + 256 * PO_STR;
constexpr int WS_END = WS_P2 + 128 * 8192;
#define STAMP(k) do {} while (0)

__device__ __forceinline__ v4u ldnt(const void* p) { return __builtin_nontemporal_load((const GAS v4u*)p); }
__device__ __forceinline__ f32x4 mfma(v4u a, v4u b, f32x4 c) {
  return __builtin_amdgcn_mfma_f32_16x16x32_bf16(__builtin_bit_cast(bf16x8, a), __builtin_bit_cast(bf16x8, b), c, 0, 0, 0);
}
__device__ __forceinline__ unsigned fbits(float f) { return __builtin_bit_cast(unsigned, f); }
__device__ __forceinline__ float bitsf(unsigned u) { return __builtin_bit_cast(float, u); }
__device__ __forceinline__ float bflo(unsigned u) { return bitsf(u << 16); }
__device__ __forceinline__ float bfhi(unsigned u) { return bitsf(u & 0xffff0000u); }
__device__ __forceinline__ unsigned cvtpk(float lo, float hi) {
  f32x2 v = {lo, hi};
  return __builtin_bit_cast(unsigned, __builtin_convertvector(v, bf16x2_t));
}
__device__ __forceinline__ float ror8(float x) {
  return bitsf(__builtin_amdgcn_mov_dpp((int)fbits(x), 0x128, 0xf, 0xf, true));
}
__device__ __forceinline__ float swap32_add(float x, float y) {
  auto r = __builtin_amdgcn_permlane32_swap(fbits(x), fbits(y), false, false);
  return bitsf(r[0]) + bitsf(r[1]);
}
__device__ __forceinline__ float swap16_add(float x, float y) {
  auto r = __builtin_amdgcn_permlane16_swap(fbits(x), fbits(y), false, false);
  return bitsf(r[0]) + bitsf(r[1]);
}
__device__ __forceinline__ float swap32_max(float x) {
  auto r = __builtin_amdgcn_permlane32_swap(fbits(x), fbits(x), false, false);
  return fmaxf(bitsf(r[0]), bitsf(r[1]));
}
__device__ __forceinline__ float swap16_max(float x) {
  auto r = __builtin_amdgcn_permlane16_swap(fbits(x), fbits(x), false, false);
  return fmaxf(bitsf(r[0]), bitsf(r[1]));
}
__device__ __forceinline__ float red4_hi(const float (&v)[4]) {
  const float u0 = swap32_add(v[0], v[2]), u1 = swap32_add(v[1], v[3]);
  const float z = swap16_add(u0, u1);
  return z + ror8(z);
}

constexpr unsigned TM = 0x3ffu;
__device__ __forceinline__ v4u tag4u(unsigned a, unsigned b, unsigned c, unsigned d, v4u tg) {
  return v4u{(a & ~TM) | tg[0], (b & ~TM) | tg[1], (c & ~TM) | tg[2], (d & ~TM) | tg[3]};
}
__device__ __forceinline__ v4u tag4(float a, float b, float c, float d, v4u tg) { return tag4u(fbits(a), fbits(b), fbits(c), fbits(d), tg); }
__device__ __forceinline__ bool ok4(v4u x, v4u tg) { return ((((x[0] ^ tg[0]) | (x[1] ^ tg[1])) | ((x[2] ^ tg[2]) | (x[3] ^ tg[3]))) & TM) == 0u; }
__device__ __forceinline__ float val(unsigned x) { return bitsf(x & ~TM); }
__device__ __forceinline__ float val2(unsigned hi, unsigned lo) { return bitsf((hi & ~TM) | ((lo >> 10) & TM)); }
__device__ __forceinline__ void put4(__amdgpu_buffer_rsrc_t rs, int off, v4u v) { __builtin_amdgcn_raw_buffer_store_b128(v, rs, off, 0, AUX_SC1); }
__device__ __forceinline__ v4u get4(__amdgpu_buffer_rsrc_t rs, int off) { return __builtin_amdgcn_raw_buffer_load_b128(rs, off, 0, AUX_SC1); }
template <int N> __device__ __forceinline__ void settle(__amdgpu_buffer_rsrc_t rs, const int (&off)[N], v4u (&x)[N], v4u tg) {
  bool ok = true;
#pragma unroll
  for (int k = 0; k < N; ++k) ok &= ok4(x[k], tg);
  if (__builtin_expect(!__all(ok), 0)) {
#pragma unroll 1
    for (unsigned spins = 0; spins < kSpinCap; ++spins) {
      __builtin_amdgcn_s_sleep(1);
      asm volatile("" ::: "memory");
      if (!ok) {
#pragma unroll
        for (int k = 0; k < N; ++k) x[k] = get4(rs, off[k]);
        ok = true;
#pragma unroll
        for (int k = 0; k < N; ++k) ok &= ok4(x[k], tg);
      }
      if (__all(ok)) break;
    }
  }
}
__device__ __forceinline__ bool last_wave(unsigned* cnt, int lane) {
  unsigned old = 0;
  asm volatile("s_waitcnt lgkmcnt(0)" ::: "memory");
  if (lane == 0) old = __hip_atomic_fetch_add((LAS unsigned*)cnt, 1u, RLX_WG);
  const bool last = __builtin_amdgcn_readfirstlane(old) == NWAVE - 1;
  asm volatile("" ::: "memory");
  return last;
}

struct Lds {
  float red[NWAVE][8][128];
  float qred[NWAVE][128];
  unsigned qs[512];
  float rml[NWAVE][8][2];
  float fin[NWAVE][32];
  float mw[8][NWAVE];
  unsigned short ov[2][32];
  unsigned qready, qcnt, mlcnt;
};

__global__ void __launch_bounds__(NTHR, 2)
gqa_decode_fused(const unsigned short* __restrict__ x, const unsigned short* __restrict__ kc, const unsigned short* __restrict__ vc,
                 const unsigned short* __restrict__ wq, const unsigned short* __restrict__ wo, unsigned short* __restrict__ out,
                 unsigned char* ws) {
  extern __shared__ __attribute__((aligned(16))) unsigned char smem[];
  Lds& L = *(Lds*)smem;
  STAMP(0);
  const int w = blockIdx.x, t = threadIdx.x, lane = t & 63, wv = __builtin_amdgcn_readfirstlane(t >> 6);
  const unsigned tb = (unsigned)__builtin_amdgcn_s_memrealtime() + T0 + wv * (TAU / 8);
  const int p = w & 7, s = w >> 3, b = p >> 2, kvh = p & 3;
  const int cgq = w >> 2, kq = w & 3;
  const int rb = w >> 1;
  const int cb = (w & 1) ? ECB + wv - 2 : wv - 2;
  const bool oproj = wv >= 2 && cb < ((w & 1) ? 8 : ECB), comb = wv < 2;
  const int a = lane & 15, g = lane >> 4;
  const __amdgpu_buffer_rsrc_t rs = __builtin_amdgcn_make_buffer_rsrc((void*)ws, 0, WS_END, 0x00020000);

  int vzero;
  asm volatile("v_mov_b32 %0, 0" : "=v"(vzero));
  const v2u epoch = __builtin_amdgcn_raw_buffer_load_b64(rs, WS_EPOCH + vzero, 0, AUX_SC1);
  const int r0 = 256 * kq + 32 * wv + 4 * (lane >> 3);
  const v2u xa = *(const GAS v2u*)(x + r0), xb = *(const GAS v2u*)(x + 1024 + r0);
  v4u W[4];
#pragma unroll
  for (int i = 0; i < 4; ++i) W[i] = ldnt(wq + (size_t)(r0 + i) * 4096 + 64 * cgq + 8 * (lane & 7));
  if (t == 0) { L.qready = 0u; L.qcnt = 0u; L.mlcnt = 0u; }
  PIN();
  __syncthreads();
  PIN();
  const size_t base = ((size_t)p * SK + (size_t)s * 1024 + wv * 16) * (DH * 2);
  const int kap = a >> 1;
  const unsigned char* kb = (const unsigned char*)kc + base + (4 * (kap & 1) + (kap >> 1)) * 256 + (2 * g + (a & 1)) * 16;
  const unsigned char* vb = (const unsigned char*)vc + base + lane * 16;
  v4u R[4][8];
#define GOFF(n) (((n) >> 1) * 32768 + ((n) & 1) * 2048)
#define ISSUE_K(n) do { R[(n) >> 2][2 * ((n) & 3)] = ldnt(kb + GOFF(n)); R[(n) >> 2][2 * ((n) & 3) + 1] = ldnt(kb + GOFF(n) + 128); PIN(); } while (0)
#define ISSUE_V(n) do { R[(n) >> 2][2 * ((n) & 3)] = ldnt(vb + GOFF(n)); R[(n) >> 2][2 * ((n) & 3) + 1] = ldnt(vb + GOFF(n) + 1024); PIN(); } while (0)
#pragma unroll
  for (int n = 0; n < NPRE; ++n) ISSUE_K(n);

  {
    float acc[2][8];
#pragma unroll
    for (int j = 0; j < 8; ++j) { acc[0][j] = 0.f; acc[1][j] = 0.f; }
#pragma unroll
    for (int i = 0; i < 4; ++i) {
      const float x0 = (i & 1) ? bfhi(xa[i >> 1]) : bflo(xa[i >> 1]);
      const float x1 = (i & 1) ? bfhi(xb[i >> 1]) : bflo(xb[i >> 1]);
#pragma unroll
      for (int u = 0; u < 4; ++u) {
        const float wa = bflo(W[i][u]), wb = bfhi(W[i][u]);
        acc[0][2 * u] += x0 * wa; acc[0][2 * u + 1] += x0 * wb;
        acc[1][2 * u] += x1 * wa; acc[1][2 * u + 1] += x1 * wb;
      }
    }
    float v8[8], v4[4], v2[2];
#pragma unroll
    for (int j = 0; j < 8; ++j) v8[j] = swap32_add(acc[0][j], acc[1][j]);
#pragma unroll
    for (int j = 0; j < 4; ++j) v4[j] = swap16_add(v8[j], v8[j + 4]);
    const bool b3 = lane & 8;
#pragma unroll
    for (int j = 0; j < 2; ++j) {
      const float keep = b3 ? v4[j + 2] : v4[j], send = b3 ? v4[j] : v4[j + 2];
      v2[j] = keep + ror8(send);
    }
    const int col = 8 * (lane & 7) + 4 * ((lane >> 4) & 1) + 2 * ((lane >> 3) & 1);
    *(f32x2*)&L.qred[wv][(lane >> 5) * 64 + col] = f32x2{v2[0], v2[1]};
  }
  STAMP(1);
  const v4u tg = {((epoch[0] & TM) * 5u + 0x147u) & TM, (((epoch[0] >> 10) & TM) * 5u + 0x2b5u) & TM,
                  ((epoch[1] & TM) * 5u + 0x09du) & TM, (((epoch[1] >> 10) & TM) * 5u + 0x3a9u) & TM};
  if (last_wave(&L.qcnt, lane)) {
    if (lane < 32) {
      f32x4 sum = *(const f32x4*)&L.qred[0][4 * lane];
#pragma unroll
      for (int ww = 1; ww < NWAVE; ++ww) sum += *(const f32x4*)&L.qred[ww][4 * lane];
      put4(rs, WS_QP + w * 512 + lane * 16, tag4(sum[0], sum[1], sum[2], sum[3], tg));
    }
  }
  PIN();
  STAMP(2);
  const int oq = WS_QP + ((16 * kvh + 2 * wv + (lane >> 5)) * 4 + 2 * (g & 1)) * 512 + b * 256 + a * 16;
  const int offq[2] = {oq, oq + 512};
  v4u xq[2];
#pragma unroll
  for (int n = NPRE; n < 16; ++n) {
    PACE(n); ISSUE_K(n);
    if (n == QPOLL_AT) { xq[0] = get4(rs, offq[0]); xq[1] = get4(rs, offq[1]); PIN(); }
    if (n == QPOLL_AT + 1) {
      settle<2>(rs, offq, xq, tg);
      float q4[4];
#pragma unroll
      for (int e = 0; e < 4; ++e) { const float v = val(xq[0][e]) + val(xq[1][e]); q4[e] = swap16_add(v, v); }
      if (!(g & 1)) *(v2u*)&L.qs[wv * 64 + (lane >> 5) * 32 + 2 * a] = v2u{cvtpk(q4[0], q4[1]), cvtpk(q4[2], q4[3])};
      asm volatile("s_waitcnt lgkmcnt(0)" ::: "memory");
      if (lane == 0) __hip_atomic_fetch_add((LAS unsigned*)&L.qready, 1u, RLX_WG);
      PIN();
      STAMP(3);
    }
  }
  while (__hip_atomic_load((LAS unsigned*)&L.qready, RLX_WG) != NWAVE) __builtin_amdgcn_s_sleep(1);
  PIN();
  STAMP(4);
  v4u qf[2];
#pragma unroll
  for (int dh = 0; dh < 2; ++dh) qf[dh] = *(const v4u*)&L.qs[(a & 7) * 64 + 32 * dh + 8 * g + 4 * (a >> 3)];

  float S[4][8];
  const bool cp = a & 8;
#pragma unroll
  for (int i = 0; i < 4; ++i) {
#pragma unroll
    for (int gm = 0; gm < 4; ++gm) {
      f32x4 c = {0.f, 0.f, 0.f, 0.f};
      c = mfma(R[i][2 * gm], qf[0], c);
      c = mfma(R[i][2 * gm + 1], qf[1], c);
      const float u0 = cp ? c[1] : c[0], u1 = cp ? c[3] : c[2];
      S[i][2 * gm] = u0 + ror8(u0);
      S[i][2 * gm + 1] = u1 + ror8(u1);
    }
    PIN();
    if (i == 0) { ISSUE_V(0); ISSUE_V(1); }
    if (i == 3) { ISSUE_V(2); ISSUE_V(3); }
  }
  STAMP(5);
  float m = S[0][0];
#pragma unroll
  for (int i = 0; i < 4; ++i)
#pragma unroll
    for (int j = 0; j < 8; ++j) m = fmaxf(m, S[i][j]);
  m = swap16_max(m);
  m = swap32_max(m);
  const float mc = m * C2;
  float l = 0.f;
  v4u P[4];
#pragma unroll
  for (int i = 0; i < 4; ++i) {
    float e[8];
#pragma unroll
    for (int j = 0; j < 8; ++j) { e[j] = __builtin_amdgcn_exp2f(S[i][j] * C2 - mc); l += e[j]; }
    P[i] = v4u{cvtpk(e[0], e[1]), cvtpk(e[2], e[3]), cvtpk(e[4], e[5]), cvtpk(e[6], e[7])};
  }
  l = swap16_add(l, l);
  l = swap32_add(l, l);
  if (lane < 8) { L.rml[wv][lane][0] = mc; L.rml[wv][lane][1] = l; }
  if (last_wave(&L.mlcnt, lane)) {
    if (lane < 8) {
      float M = L.rml[0][lane][0];
#pragma unroll
      for (int ww = 1; ww < NWAVE; ++ww) M = fmaxf(M, L.rml[ww][lane][0]);
      float Ls = 0.f;
#pragma unroll
      for (int ww = 0; ww < NWAVE; ++ww) {
        const float f = __builtin_amdgcn_exp2f(L.rml[ww][lane][0] - M);
        L.mw[lane][ww] = f;
        Ls += f * L.rml[ww][lane][1];
      }
      put4(rs, WS_PO + w * PO_STR + 4096 + lane * 16, tag4u(fbits(M), fbits(Ls), fbits(M) << 10, fbits(Ls) << 10, tg));
    }
  }
  PIN();
  f32x4 O[8];
#pragma unroll
  for (int e = 0; e < 8; ++e) O[e] = f32x4{0.f, 0.f, 0.f, 0.f};
  const int po0 = WS_PO + ((lane >> 3) * 8 + (wv & 1) * 4 + (rb >> 5)) * PO_STR;
  const int om = po0 + 4096 + ((rb >> 2) & 7) * 16;
  const int offm[4] = {om, om + 64 * PO_STR, om + 128 * PO_STR, om + 192 * PO_STR};
  v4u ML[4] = {};
#pragma unroll
  for (int i = 0; i < 4; ++i) {
    if (i == 0) { PACE(16 + 4); ISSUE_V(4); PACE(16 + 5); ISSUE_V(5); PACE(16 + 6); ISSUE_V(6); }
    if (i == 1) { PACE(16 + 7); ISSUE_V(7); PACE(16 + 8); ISSUE_V(8); PACE(16 + 9); ISSUE_V(9); PACE(16 + 10); ISSUE_V(10); }
    if (i == 2) {
      PACE(16 + 11); ISSUE_V(11); PACE(16 + 12); ISSUE_V(12);
      if (comb) {
#pragma unroll
        for (int k = 0; k < 4; ++k) ML[k] = get4(rs, offm[k]);
      }
      PIN();
      PACE(16 + 13); ISSUE_V(13); PACE(16 + 14); ISSUE_V(14); PACE(16 + 15); ISSUE_V(15);
    }
#pragma unroll
    for (int e = 0; e < 8; ++e) {
      const unsigned sel = (e & 1) ? 0x07060302u : 0x05040100u;
      v4u bv;
#pragma unroll
      for (int u = 0; u < 4; ++u) bv[u] = __builtin_amdgcn_perm(R[i][2 * u + 1][e >> 1], R[i][2 * u][e >> 1], sel);
      O[e] = mfma(P[i], bv, O[e]);
    }
    PIN();
  }
  STAMP(6);
  v4u WO[8];
  if (oproj) {
#pragma unroll
    for (int j = 0; j < 8; ++j) WO[j] = ldnt(wo + (size_t)(32 * rb + 8 * g + j) * 1024 + 128 * cb + 8 * a);
  }
  PIN();
  if (g < 2) {
#pragma unroll
    for (int r = 0; r < 4; ++r) {
      *(f32x4*)&L.red[wv][4 * g + r][8 * a] = f32x4{O[0][r], O[1][r], O[2][r], O[3][r]};
      *(f32x4*)&L.red[wv][4 * g + r][8 * a + 4] = f32x4{O[4][r], O[5][r], O[6][r], O[7][r]};
    }
  }
  __syncthreads();
  if (lane < 32) {
    const f32x4 f0 = *(const f32x4*)&L.mw[wv][0], f1 = *(const f32x4*)&L.mw[wv][4];
    f32x4 pp[NWAVE];
#pragma unroll
    for (int ww = 0; ww < NWAVE; ++ww) pp[ww] = *(const f32x4*)&L.red[ww][wv][4 * lane];
    __builtin_amdgcn_sched_barrier(0);
    const f32x4 o = ((f0[0] * pp[0] + f0[1] * pp[1]) + (f0[2] * pp[2] + f0[3] * pp[3])) + ((f1[0] * pp[4] + f1[1] * pp[5]) + (f1[2] * pp[6] + f1[3] * pp[7]));
    put4(rs, WS_PO + w * PO_STR + wv * 512 + lane * 16, tag4(o[0], o[1], o[2], o[3], tg));
  }
  PIN();
  STAMP(7);
  v4u BW[8];
  if (comb) {
    const int oo = po0 + ((rb >> 2) & 7) * 512 + (rb & 3) * 128 + (lane & 7) * 16;
    const int off[4] = {oo, oo + 64 * PO_STR, oo + 128 * PO_STR, oo + 192 * PO_STR};
    v4u xx[4];
    __builtin_amdgcn_s_sleep(NAP1);
#pragma unroll
    for (int k = 0; k < 4; ++k) xx[k] = get4(rs, off[k]);
    PIN();
    float wt[4];
    settle<4>(rs, offm, ML, tg);
    float ms[4];
#pragma unroll
    for (int k = 0; k < 4; ++k) ms[k] = val2(ML[k][0], ML[k][2]);
    float M = fmaxf(fmaxf(ms[0], ms[1]), fmaxf(ms[2], ms[3]));
    M = fmaxf(M, ror8(M));
    M = swap16_max(M); M = swap32_max(M);
    float Ls = 0.f;
#pragma unroll
    for (int k = 0; k < 4; ++k) { wt[k] = __builtin_amdgcn_exp2f(ms[k] - M); Ls += wt[k] * val2(ML[k][1], ML[k][3]); }
    Ls = swap32_add(Ls, Ls); Ls = swap16_add(Ls, Ls); Ls += ror8(Ls);
    const float inv = 1.0f / Ls;
#pragma unroll
    for (int k = 0; k < 4; ++k) { wt[k] *= inv; asm volatile("" : "+v"(wt[k])); }
    PIN();
    settle<4>(rs, off, xx, tg);
    float o[4];
#pragma unroll
    for (int e = 0; e < 4; ++e) o[e] = (wt[0] * val(xx[0][e]) + wt[1] * val(xx[1][e])) + (wt[2] * val(xx[2][e]) + wt[3] * val(xx[3][e]));
    const unsigned pk = cvtpk(red4_hi(o), 0.f);
    if ((lane & 8) == 0) L.ov[wv][4 * (lane & 7) + 2 * (lane >> 5) + ((lane >> 4) & 1)] = (unsigned short)pk;
  } else if (oproj) {
#pragma unroll
    for (int e = 0; e < 8; ++e)
#pragma unroll
      for (int u = 0; u < 4; ++u) BW[e][u] = __builtin_amdgcn_perm(WO[2 * u + 1][e >> 1], WO[2 * u][e >> 1], (e & 1) ? 0x07060302u : 0x05040100u);
#pragma unroll
    for (int e = 0; e < 8; ++e) asm volatile("" : "+v"(BW[e]));
  }
  PIN();
  STAMP(8);
  __syncthreads();
  if (oproj) {
    const v4u ao = *(const v4u*)&L.ov[(a >> 2) & 1][8 * g];
    f32x4 d[8];
#pragma unroll
    for (int e = 0; e < 8; ++e) d[e] = mfma(ao, BW[e], f32x4{0.f, 0.f, 0.f, 0.f});
    const bool hi = g >> 1;
    put4(rs, WS_P2 + rb * 8192 + (g & 1) * 4096 + cb * 512 + a * 32 + (g >> 1) * 16,
         tag4(hi ? d[4][0] : d[0][0], hi ? d[5][0] : d[1][0], hi ? d[6][0] : d[2][0], hi ? d[7][0] : d[3][0], tg));
  }
  PIN();
  STAMP(9);
  if ((w & 1) && w < 128) {
    const int o0 = WS_P2 + (16 * wv + (lane >> 3)) * 8192 + rb * 128 + (lane & 7) * 16;
    const int of2[2] = {o0, o0 + 8 * 8192};
    __builtin_amdgcn_s_sleep(NAP2);
    v4u yy[2] = {get4(rs, of2[0]), get4(rs, of2[1])};
    settle<2>(rs, of2, yy, tg);
    float v[4];
#pragma unroll
    for (int e = 0; e < 4; ++e) v[e] = val(yy[0][e]) + val(yy[1][e]);
    const float z = red4_hi(v);
    if ((lane & 8) == 0) L.fin[wv][4 * (lane & 7) + 2 * (lane >> 5) + ((lane >> 4) & 1)] = z;
    STAMP(10);
    __syncthreads();
    if (t < 16) {
      f32x2 sm = *(const f32x2*)&L.fin[0][2 * t];
#pragma unroll
      for (int ww = 1; ww < NWAVE; ++ww) sm += *(const f32x2*)&L.fin[ww][2 * t];
      ((unsigned*)out)[rb * 16 + t] = cvtpk(sm[0], sm[1]);
    }
    if (w == 1 && t == 0) __builtin_amdgcn_raw_buffer_store_b64(v2u{tg[0] | (tg[1] << 10), tg[2] | (tg[3] << 10)}, rs, WS_EPOCH, 0, AUX_SC1);
  } else { STAMP(10); }
}

extern "C" void kernel_launch(void* const* d_in, const int* in_sizes, int n_in, void* d_out, int out_size, void* d_ws,
                              size_t ws_size, hipStream_t stream) {
  constexpr int kLds = 96 * 1024;
  static_assert(sizeof(Lds) <= kLds, "LDS");
  (void)hipFuncSetAttribute((const void*)gqa_decode_fused, hipFuncAttributeMaxDynamicSharedMemorySize, kLds);
  gqa_decode_fused<<<NWG, NTHR, kLds, stream>>>((const unsigned short*)d_in[0], (const unsigned short*)d_in[1],
                                                (const unsigned short*)d_in[2], (const unsigned short*)d_in[3],
                                                (const unsigned short*)d_in[4], (unsigned short*)d_out, (unsigned char*)d_ws);
}
```

```cpp
#include <hip/hip_runtime.h>
#include <hip/hip_bf16.h>
#include <stdint.h>

typedef unsigned v4u __attribute__((ext_vector_type(4)));
typedef unsigned v2u __attribute__((ext_vector_type(2)));
typedef float f32x4 __attribute__((ext_vector_type(4)));
typedef float f32x2 __attribute__((ext_vector_type(2)));
typedef short bf16x8 __attribute__((ext_vector_type(8)));
typedef __bf16 bf16x2_t __attribute__((ext_vector_type(2)));
#define GAS __attribute__((address_space(1)))
#define LAS __attribute__((address_space(3)))
#define RLX_WG __ATOMIC_RELAXED, __HIP_MEMORY_SCOPE_WORKGROUP

constexpr int SK = 32768, DH = 128;
constexpr int NWG = 256, NTHR = 512, NWAVE = 8;
constexpr float C2 = 0.08838834764831845f * 1.4426950408889634f;
constexpr unsigned kSpinCap = 1u << 17;
constexpr int NPRE = 4;
constexpr int QPOLL_AT = 10;
constexpr int ECB = 6;
constexpr int AUX_SC1 = 16;

constexpr int T0 = 30, TAU = 67;
#define PACE(n) do { asm volatile("s_waitcnt vmcnt(2)" ::: "memory"); \
    while ((int)((unsigned)__builtin_amdgcn_s_memrealtime() - (tb + (unsigned)(((n) - 4) * TAU))) < 0) __builtin_amdgcn_s_sleep(1); } while (0)
constexpr int NAP1 = 26, NAP2 = 24;
#define PIN() do { asm volatile("" ::: "memory"); __builtin_amdgcn_sched_barrier(0); } while (0)

constexpr int WS_EPOCH = 0;
constexpr int WS_QP = 4096;
constexpr int WS_PO = WS_QP + 256 * 512;
constexpr int PO_STR = 4096 + 128;
constexpr int WS_P2 = WS_PO + 256 * PO_STR;
constexpr int WS_END = WS_P2 + 128 * 8192;

__device__ __forceinline__ v4u ldnt(const void* p) { return __builtin_nontemporal_load((const GAS v4u*)p); }
__device__ __forceinline__ f32x4 mfma(v4u a, v4u b, f32x4 c) {
  return __builtin_amdgcn_mfma_f32_16x16x32_bf16(__builtin_bit_cast(bf16x8, a), __builtin_bit_cast(bf16x8, b), c, 0, 0, 0);
}
__device__ __forceinline__ unsigned fbits(float f) { return __builtin_bit_cast(unsigned, f); }
__device__ __forceinline__ float bitsf(unsigned u) { return __builtin_bit_cast(float, u); }
__device__ __forceinline__ float bflo(unsigned u) { return bitsf(u << 16); }
__device__ __forceinline__ float bfhi(unsigned u) { return bitsf(u & 0xffff0000u); }
__device__ __forceinline__ unsigned cvtpk(float lo, float hi) {
  f32x2 v = {lo, hi};
  return __builtin_bit_cast(unsigned, __builtin_convertvector(v, bf16x2_t));
}
__device__ __forceinline__ float ror8(float x) {
  return bitsf(__builtin_amdgcn_mov_dpp((int)fbits(x), 0x128, 0xf, 0xf, true));
}
__device__ __forceinline__ float swap32_add(float x, float y) {
  auto r = __builtin_amdgcn_permlane32_swap(fbits(x), fbits(y), false, false);
  return bitsf(r[0]) + bitsf(r[1]);
}
__device__ __forceinline__ float swap16_add(float x, float y) {
  auto r = __builtin_amdgcn_permlane16_swap(fbits(x), fbits(y), false, false);
  return bitsf(r[0]) + bitsf(r[1]);
}
__device__ __forceinline__ float swap32_max(float x) {
  auto r = __builtin_amdgcn_permlane32_swap(fbits(x), fbits(x), false, false);
  return fmaxf(bitsf(r[0]), bitsf(r[1]));
}
__device__ __forceinline__ float swap16_max(float x) {
  auto r = __builtin_amdgcn_permlane16_swap(fbits(x), fbits(x), false, false);
  return fmaxf(bitsf(r[0]), bitsf(r[1]));
}
__device__ __forceinline__ float red4_hi(const float (&v)[4]) {
  const float u0 = swap32_add(v[0], v[2]), u1 = swap32_add(v[1], v[3]);
  const float z = swap16_add(u0, u1);
  return z + ror8(z);
}

constexpr unsigned TM = 0x3ffu;
__device__ __forceinline__ v4u tag4u(unsigned a, unsigned b, unsigned c, unsigned d, v4u tg) {
  return v4u{(a & ~TM) | tg[0], (b & ~TM) | tg[1], (c & ~TM) | tg[2], (d & ~TM) | tg[3]};
}
__device__ __forceinline__ v4u tag4(float a, float b, float c, float d, v4u tg) { return tag4u(fbits(a), fbits(b), fbits(c), fbits(d), tg); }
__device__ __forceinline__ bool ok4(v4u x, v4u tg) { return ((((x[0] ^ tg[0]) | (x[1] ^ tg[1])) | ((x[2] ^ tg[2]) | (x[3] ^ tg[3]))) & TM) == 0u; }
__device__ __forceinline__ float val(unsigned x) { return bitsf(x & ~TM); }
__device__ __forceinline__ float val2(unsigned hi, unsigned lo) { return bitsf((hi & ~TM) | ((lo >> 10) & TM)); }
__device__ __forceinline__ void put4(__amdgpu_buffer_rsrc_t rs, int off, v4u v) { __builtin_amdgcn_raw_buffer_store_b128(v, rs, off, 0, AUX_SC1); }
__device__ __forceinline__ v4u get4(__amdgpu_buffer_rsrc_t rs, int off) { return __builtin_amdgcn_raw_buffer_load_b128(rs, off, 0, AUX_SC1); }
template <int N> __device__ __forceinline__ void settle(__amdgpu_buffer_rsrc_t rs, const int (&off)[N], v4u (&x)[N], v4u tg) {
  bool ok = true;
#pragma unroll
  for (int k = 0; k < N; ++k) ok &= ok4(x[k], tg);
  if (__builtin_expect(!__all(ok), 0)) {
#pragma unroll 1
    for (unsigned spins = 0; spins < kSpinCap; ++spins) {
      __builtin_amdgcn_s_sleep(1);
      asm volatile("" ::: "memory");
      if (!ok) {
#pragma unroll
        for (int k = 0; k < N; ++k) x[k] = get4(rs, off[k]);
        ok = true;
#pragma unroll
        for (int k = 0; k < N; ++k) ok &= ok4(x[k], tg);
      }
      if (__all(ok)) break;
    }
  }
}
__device__ __forceinline__ bool last_wave(unsigned* cnt, int lane) {
  unsigned old = 0;
  asm volatile("s_waitcnt lgkmcnt(0)" ::: "memory");
  if (lane == 0) old = __hip_atomic_fetch_add((LAS unsigned*)cnt, 1u, RLX_WG);
  const bool last = __builtin_amdgcn_readfirstlane(old) == NWAVE - 1;
  asm volatile("" ::: "memory");
  return last;
}

struct Lds {
  float red[NWAVE][8][128];
  float qred[NWAVE][128];
  unsigned qs[512];
  float rml[NWAVE][8][2];
  float fin[NWAVE][32];
  float mw[8][NWAVE];
  unsigned short ov[2][32];
  unsigned qready, qcnt, mlcnt;
};

__global__ void __launch_bounds__(NTHR, 2)
gqa_decode_fused(const unsigned short* __restrict__ x, const unsigned short* __restrict__ kc, const unsigned short* __restrict__ vc,
                 const unsigned short* __restrict__ wq, const unsigned short* __restrict__ wo, unsigned short* __restrict__ out,
                 unsigned char* ws) {
  extern __shared__ __attribute__((aligned(16))) unsigned char smem[];
  Lds& L = *(Lds*)smem;
  const int w = blockIdx.x, t = threadIdx.x, lane = t & 63, wv = __builtin_amdgcn_readfirstlane(t >> 6);
  const unsigned tb = (unsigned)__builtin_amdgcn_s_memrealtime() + T0 + wv * (TAU / 8);
  const int p = w & 7, s = w >> 3, b = p >> 2, kvh = p & 3;
  const int cgq = w >> 2, kq = w & 3;
  const int rb = w >> 1;
  const int cb = (w & 1) ? ECB + wv - 2 : wv - 2;
  const bool oproj = wv >= 2 && cb < ((w & 1) ? 8 : ECB), comb = wv < 2;
  const int a = lane & 15, g = lane >> 4;
  const __amdgpu_buffer_rsrc_t rs = __builtin_amdgcn_make_buffer_rsrc((void*)ws, 0, WS_END, 0x00020000);

  int vzero;
  asm volatile("v_mov_b32 %0, 0" : "=v"(vzero));
  const v2u epoch = __builtin_amdgcn_raw_buffer_load_b64(rs, WS_EPOCH + vzero, 0, AUX_SC1);
  const int r0 = 256 * kq + 32 * wv + 4 * (lane >> 3);
  const v2u xa = *(const GAS v2u*)(x + r0), xb = *(const GAS v2u*)(x + 1024 + r0);
  v4u W[4];
#pragma unroll
  for (int i = 0; i < 4; ++i) W[i] = ldnt(wq + (size_t)(r0 + i) * 4096 + 64 * cgq + 8 * (lane & 7));
  if (t == 0) { L.qready = 0u; L.qcnt = 0u; L.mlcnt = 0u; }
  PIN();
  __syncthreads();
  PIN();
  const size_t base = ((size_t)p * SK + (size_t)s * 1024 + wv * 16) * (DH * 2);
  const int kap = a >> 1;
  const unsigned char* kb = (const unsigned char*)kc + base + (4 * (kap & 1) + (kap >> 1)) * 256 + (2 * g + (a & 1)) * 16;
  const unsigned char* vb = (const unsigned char*)vc + base + lane * 16;
  v4u R[4][8];
#define GOFF(n) (((n) >> 1) * 32768 + ((n) & 1) * 2048)
#define ISSUE_K(n) do { R[(n) >> 2][2 * ((n) & 3)] = ldnt(kb + GOFF(n)); R[(n) >> 2][2 * ((n) & 3) + 1] = ldnt(kb + GOFF(n) + 128); PIN(); } while (0)
#define ISSUE_V(n) do { R[(n) >> 2][2 * ((n) & 3)] = ldnt(vb + GOFF(n)); R[(n) >> 2][2 * ((n) & 3) + 1] = ldnt(vb + GOFF(n) + 1024); PIN(); } while (0)
#pragma unroll
  for (int n = 0; n < NPRE; ++n) ISSUE_K(n);

  {
    float acc[2][8];
#pragma unroll
    for (int j = 0; j < 8; ++j) { acc[0][j] = 0.f; acc[1][j] = 0.f; }
#pragma unroll
    for (int i = 0; i < 4; ++i) {
      const float x0 = (i & 1) ? bfhi(xa[i >> 1]) : bflo(xa[i >> 1]);
      const float x1 = (i & 1) ? bfhi(xb[i >> 1]) : bflo(xb[i >> 1]);
#pragma unroll
      for (int u = 0; u < 4; ++u) {
        const float wa = bflo(W[i][u]), wb = bfhi(W[i][u]);
        acc[0][2 * u] += x0 * wa; acc[0][2 * u + 1] += x0 * wb;
        acc[1][2 * u] += x1 * wa; acc[1][2 * u + 1] += x1 * wb;
      }
    }
    float v8[8], v4[4], v2[2];
#pragma unroll
    for (int j = 0; j < 8; ++j) v8[j] = swap32_add(acc[0][j], acc[1][j]);
#pragma unroll
    for (int j = 0; j < 4; ++j) v4[j] = swap16_add(v8[j], v8[j + 4]);
    const bool b3 = lane & 8;
#pragma unroll
    for (int j = 0; j < 2; ++j) {
      const float keep = b3 ? v4[j + 2] : v4[j], send = b3 ? v4[j] : v4[j + 2];
      v2[j] = keep + ror8(send);
    }
    const int col = 8 * (lane & 7) + 4 * ((lane >> 4) & 1) + 2 * ((lane >> 3) & 1);
    *(f32x2*)&L.qred[wv][(lane >> 5) * 64 + col] = f32x2{v2[0], v2[1]};
  }
  const v4u tg = {((epoch[0] & TM) * 5u + 0x147u) & TM, (((epoch[0] >> 10) & TM) * 5u + 0x2b5u) & TM,
                  ((epoch[1] & TM) * 5u + 0x09du) & TM, (((epoch[1] >> 10) & TM) * 5u + 0x3a9u) & TM};
  if (last_wave(&L.qcnt, lane)) {
    if (lane < 32) {
      f32x4 sum = *(const f32x4*)&L.qred[0][4 * lane];
#pragma unroll
      for (int ww = 1; ww < NWAVE; ++ww) sum += *(const f32x4*)&L.qred[ww][4 * lane];
      put4(rs, WS_QP + w * 512 + lane * 16, tag4(sum[0], sum[1], sum[2], sum[3], tg));
    }
  }
  PIN();
  const int oq = WS_QP + ((16 * kvh + 2 * wv + (lane >> 5)) * 4 + 2 * (g & 1)) * 512 + b * 256 + a * 16;
  const int offq[2] = {oq, oq + 512};
  v4u xq[2];
#pragma unroll
  for (int n = NPRE; n < 16; ++n) {
    PACE(n); ISSUE_K(n);
    if (n == QPOLL_AT) { xq[0] = get4(rs, offq[0]); xq[1] = get4(rs, offq[1]); PIN(); }
    if (n == QPOLL_AT + 1) {
      settle<2>(rs, offq, xq, tg);
      float q4[4];
#pragma unroll
      for (int e = 0; e < 4; ++e) { const float v = val(xq[0][e]) + val(xq[1][e]); q4[e] = swap16_add(v, v); }
      if (!(g & 1)) *(v2u*)&L.qs[wv * 64 + (lane >> 5) * 32 + 2 * a] = v2u{cvtpk(q4[0], q4[1]), cvtpk(q4[2], q4[3])};
      asm volatile("s_waitcnt lgkmcnt(0)" ::: "memory");
      if (lane == 0) __hip_atomic_fetch_add((LAS unsigned*)&L.qready, 1u, RLX_WG);
      PIN();
    }
  }
  while (__hip_atomic_load((LAS unsigned*)&L.qready, RLX_WG) != NWAVE) __builtin_amdgcn_s_sleep(1);
  PIN();
  v4u qf[2];
#pragma unroll
  for (int dh = 0; dh < 2; ++dh) qf[dh] = *(const v4u*)&L.qs[(a & 7) * 64 + 32 * dh + 8 * g + 4 * (a >> 3)];

  float S[4][8];
  const bool cp = a & 8;
#pragma unroll
  for (int i = 0; i < 4; ++i) {
#pragma unroll
    for (int gm = 0; gm < 4; ++gm) {
      f32x4 c = {0.f, 0.f, 0.f, 0.f};
      c = mfma(R[i][2 * gm], qf[0], c);
      c = mfma(R[i][2 * gm + 1], qf[1], c);
      const float u0 = cp ? c[1] : c[0], u1 = cp ? c[3] : c[2];
      S[i][2 * gm] = u0 + ror8(u0);
      S[i][2 * gm + 1] = u1 + ror8(u1);
    }
    PIN();
    if (i == 0) { ISSUE_V(0); ISSUE_V(1); }
    if (i == 3) { ISSUE_V(2); ISSUE_V(3); }
  }
  float m = S[0][0];
#pragma unroll
  for (int i = 0; i < 4; ++i)
#pragma unroll
    for (int j = 0; j < 8; ++j) m = fmaxf(m, S[i][j]);
  m = swap16_max(m);
  m = swap32_max(m);
  const float mc = m * C2;
  float l = 0.f;
  v4u P[4];
#pragma unroll
  for (int i = 0; i < 4; ++i) {
    float e[8];
#pragma unroll
    for (int j = 0; j < 8; ++j) { e[j] = __builtin_amdgcn_exp2f(S[i][j] * C2 - mc); l += e[j]; }
    P[i] = v4u{cvtpk(e[0], e[1]), cvtpk(e[2], e[3]), cvtpk(e[4], e[5]), cvtpk(e[6], e[7])};
  }
  l = swap16_add(l, l);
  l = swap32_add(l, l);
  if (lane < 8) { L.rml[wv][lane][0] = mc; L.rml[wv][lane][1] = l; }
  if (last_wave(&L.mlcnt, lane)) {
    if (lane < 8) {
      float M = L.rml[0][lane][0];
#pragma unroll
      for (int ww = 1; ww < NWAVE; ++ww) M = fmaxf(M, L.rml[ww][lane][0]);
      float Ls = 0.f;
#pragma unroll
      for (int ww = 0; ww < NWAVE; ++ww) {
        const float f = __builtin_amdgcn_exp2f(L.rml[ww][lane][0] - M);
        L.mw[lane][ww] = f;
        Ls += f * L.rml[ww][lane][1];
      }
      put4(rs, WS_PO + w * PO_STR + 4096 + lane * 16, tag4u(fbits(M), fbits(Ls), fbits(M) << 10, fbits(Ls) << 10, tg));
    }
  }
  PIN();
  f32x4 O[8];
#pragma unroll
  for (int e = 0; e < 8; ++e) O[e] = f32x4{0.f, 0.f, 0.f, 0.f};
  const int po0 = WS_PO + ((lane >> 3) * 8 + (wv & 1) * 4 + (rb >> 5)) * PO_STR;
  const int om = po0 + 4096 + ((rb >> 2) & 7) * 16;
  const int offm[4] = {om, om + 64 * PO_STR, om + 128 * PO_STR, om + 192 * PO_STR};
  v4u ML[4] = {};
#pragma unroll
  for (int i = 0; i < 4; ++i) {
    if (i == 0) { PACE(16 + 4); ISSUE_V(4); PACE(16 + 5); ISSUE_V(5); PACE(16 + 6); ISSUE_V(6); }
    if (i == 1) { PACE(16 + 7); ISSUE_V(7); PACE(16 + 8); ISSUE_V(8); PACE(16 + 9); ISSUE_V(9); PACE(16 + 10); ISSUE_V(10); }
    if (i == 2) {
      PACE(16 + 11); ISSUE_V(11); PACE(16 + 12); ISSUE_V(12);
      if (comb) {
#pragma unroll
        for (int k = 0; k < 4; ++k) ML[k] = get4(rs, offm[k]);
      }
      PIN();
      PACE(16 + 13); ISSUE_V(13); PACE(16 + 14); ISSUE_V(14); PACE(16 + 15); ISSUE_V(15);
    }
#pragma unroll
    for (int e = 0; e < 8; ++e) {
      const unsigned sel = (e & 1) ? 0x07060302u : 0x05040100u;
      v4u bv;
#pragma unroll
      for (int u = 0; u < 4; ++u) bv[u] = __builtin_amdgcn_perm(R[i][2 * u + 1][e >> 1], R[i][2 * u][e >> 1], sel);
      O[e] = mfma(P[i], bv, O[e]);
    }
    PIN();
  }
  v4u WO[8];
  if (oproj) {
#pragma unroll
    for (int j = 0; j < 8; ++j) WO[j] = ldnt(wo + (size_t)(32 * rb + 8 * g + j) * 1024 + 128 * cb + 8 * a);
  }
  PIN();
  if (g < 2) {
#pragma unroll
    for (int r = 0; r < 4; ++r) {
      *(f32x4*)&L.red[wv][4 * g + r][8 * a] = f32x4{O[0][r], O[1][r], O[2][r], O[3][r]};
      *(f32x4*)&L.red[wv][4 * g + r][8 * a + 4] = f32x4{O[4][r], O[5][r], O[6][r], O[7][r]};
    }
  }
  __syncthreads();
  if (lane < 32) {
    const f32x4 f0 = *(const f32x4*)&L.mw[wv][0], f1 = *(const f32x4*)&L.mw[wv][4];
    f32x4 pp[NWAVE];
#pragma unroll
    for (int ww = 0; ww < NWAVE; ++ww) pp[ww] = *(const f32x4*)&L.red[ww][wv][4 * lane];
    __builtin_amdgcn_sched_barrier(0);
    const f32x4 o = ((f0[0] * pp[0] + f0[1] * pp[1]) + (f0[2] * pp[2] + f0[3] * pp[3])) + ((f1[0] * pp[4] + f1[1] * pp[5]) + (f1[2] * pp[6] + f1[3] * pp[7]));
    put4(rs, WS_PO + w * PO_STR + wv * 512 + lane * 16, tag4(o[0], o[1], o[2], o[3], tg));
  }
  PIN();
  v4u BW[8];
  if (comb) {
    const int oo = po0 + ((rb >> 2) & 7) * 512 + (rb & 3) * 128 + (lane & 7) * 16;
    const int off[4] = {oo, oo + 64 * PO_STR, oo + 128 * PO_STR, oo + 192 * PO_STR};
    v4u xx[4];
    __builtin_amdgcn_s_sleep(NAP1);
#pragma unroll
    for (int k = 0; k < 4; ++k) xx[k] = get4(rs, off[k]);
    PIN();
    float wt[4];
    settle<4>(rs, offm, ML, tg);
    float ms[4];
#pragma unroll
    for (int k = 0; k < 4; ++k) ms[k] = val2(ML[k][0], ML[k][2]);
    float M = fmaxf(fmaxf(ms[0], ms[1]), fmaxf(ms[2], ms[3]));
    M = fmaxf(M, ror8(M));
    M = swap16_max(M); M = swap32_max(M);
    float Ls = 0.f;
#pragma unroll
    for (int k = 0; k < 4; ++k) { wt[k] = __builtin_amdgcn_exp2f(ms[k] - M); Ls += wt[k] * val2(ML[k][1], ML[k][3]); }
    Ls = swap32_add(Ls, Ls); Ls = swap16_add(Ls, Ls); Ls += ror8(Ls);
    const float inv = 1.0f / Ls;
#pragma unroll
    for (int k = 0; k < 4; ++k) { wt[k] *= inv; asm volatile("" : "+v"(wt[k])); }
    PIN();
    settle<4>(rs, off, xx, tg);
    float o[4];
#pragma unroll
    for (int e = 0; e < 4; ++e) o[e] = (wt[0] * val(xx[0][e]) + wt[1] * val(xx[1][e])) + (wt[2] * val(xx[2][e]) + wt[3] * val(xx[3][e]));
    const unsigned pk = cvtpk(red4_hi(o), 0.f);
    if ((lane & 8) == 0) L.ov[wv][4 * (lane & 7) + 2 * (lane >> 5) + ((lane >> 4) & 1)] = (unsigned short)pk;
  } else if (oproj) {
#pragma unroll
    for (int e = 0; e < 8; ++e)
#pragma unroll
      for (int u = 0; u < 4; ++u) BW[e][u] = __builtin_amdgcn_perm(WO[2 * u + 1][e >> 1], WO[2 * u][e >> 1], (e & 1) ? 0x07060302u : 0x05040100u);
#pragma unroll
    for (int e = 0; e < 8; ++e) asm volatile("" : "+v"(BW[e]));
  }
  PIN();
  __syncthreads();
  if (oproj) {
    const v4u ao = *(const v4u*)&L.ov[(a >> 2) & 1][8 * g];
    f32x4 d[8];
#pragma unroll
    for (int e = 0; e < 8; ++e) d[e] = mfma(ao, BW[e], f32x4{0.f, 0.f, 0.f, 0.f});
    const bool hi = g >> 1;
    put4(rs, WS_P2 + rb * 8192 + (g & 1) * 4096 + cb * 512 + a * 32 + (g >> 1) * 16,
         tag4(hi ? d[4][0] : d[0][0], hi ? d[5][0] : d[1][0], hi ? d[6][0] : d[2][0], hi ? d[7][0] : d[3][0], tg));
  }
  PIN();
  if ((w & 1) && w < 128) {
    const int o0 = WS_P2 + (16 * wv + (lane >> 3)) * 8192 + rb * 128 + (lane & 7) * 16;
    const int of2[2] = {o0, o0 + 8 * 8192};
    __builtin_amdgcn_s_sleep(NAP2);
    v4u yy[2] = {get4(rs, of2[0]), get4(rs, of2[1])};
    settle<2>(rs, of2, yy, tg);
    float v[4];
#pragma unroll
    for (int e = 0; e < 4; ++e) v[e] = val(yy[0][e]) + val(yy[1][e]);
    const float z = red4_hi(v);
    if ((lane & 8) == 0) L.fin[wv][4 * (lane & 7) + 2 * (lane >> 5) + ((lane >> 4) & 1)] = z;
    __syncthreads();
    if (t < 16) {
      f32x2 sm = *(const f32x2*)&L.fin[0][2 * t];
#pragma unroll
      for (int ww = 1; ww < NWAVE; ++ww) sm += *(const f32x2*)&L.fin[ww][2 * t];
      ((unsigned*)out)[rb * 16 + t] = cvtpk(sm[0], sm[1]);
    }
    if (w == 1 && t == 0) __builtin_amdgcn_raw_buffer_store_b64(v2u{tg[0] | (tg[1] << 10), tg[2] | (tg[3] << 10)}, rs, WS_EPOCH, 0, AUX_SC1);
  }
}

extern "C" void kernel_launch(void* const* d_in, const int* in_sizes, int n_in, void* d_out, int out_size, void* d_ws,
                              size_t ws_size, hipStream_t stream) {
  constexpr int kLds = 96 * 1024;
  static_assert(sizeof(Lds) <= kLds, "LDS");
  (void)hipFuncSetAttribute((const void*)gqa_decode_fused, hipFuncAttributeMaxDynamicSharedMemorySize, kLds);
  gqa_decode_fused<<<NWG, NTHR, kLds, stream>>>((const unsigned short*)d_in[0], (const unsigned short*)d_in[1],
                                                (const unsigned short*)d_in[2], (const unsigned short*)d_in[3],
                                                (const unsigned short*)d_in[4], (unsigned short*)d_out, (unsigned char*)d_ws);
}
```

```cpp
#include <hip/hip_runtime.h>
#include <hip/hip_bf16.h>
#include <stdint.h>

typedef unsigned v4u __attribute__((ext_vector_type(4)));
typedef unsigned v2u __attribute__((ext_vector_type(2)));
typedef float f32x4 __attribute__((ext_vector_type(4)));
typedef float f32x2 __attribute__((ext_vector_type(2)));
typedef short bf16x8 __attribute__((ext_vector_type(8)));
typedef __bf16 bf16x2_t __attribute__((ext_vector_type(2)));
#define GAS __attribute__((address_space(1)))
#define LAS __attribute__((address_space(3)))
#define RLX_WG __ATOMIC_RELAXED, __HIP_MEMORY_SCOPE_WORKGROUP

constexpr int SK = 32768, DH = 128;
constexpr int NWG = 256, NTHR = 512, NWAVE = 8;
constexpr float C2 = 0.08838834764831845f * 1.4426950408889634f;
constexpr unsigned kSpinCap = 1u << 17;
constexpr int NPRE = 4;
constexpr int QPOLL_AT = 10;
constexpr int ECB = 6;
constexpr int AUX_SC1 = 16;
constexpr int CODE_BYTES = 13312, WARM_WGS = 16;

constexpr int T0 = 30, TAU = 67;
#define PACE(n) do { asm volatile("s_waitcnt vmcnt(2)" ::: "memory"); \
    while ((int)((unsigned)__builtin_amdgcn_s_memrealtime() - (tb + (unsigned)(((n) - 4) * TAU))) < 0) __builtin_amdgcn_s_sleep(1); } while (0)
constexpr int NAP1 = 26, NAP2 = 24;
#define PIN() do { asm volatile("" ::: "memory"); __builtin_amdgcn_sched_barrier(0); } while (0)

constexpr int WS_EPOCH = 0;
constexpr int WS_QP = 4096;
constexpr int WS_PO = WS_QP + 256 * 512;
constexpr int PO_STR = 4096 + 128;
constexpr int WS_P2 = WS_PO + 256 * PO_STR;
constexpr int WS_END = WS_P2 + 128 * 8192;

__device__ __forceinline__ v4u ldnt(const void* p) { return __builtin_nontemporal_load((const GAS v4u*)p); }
__device__ __forceinline__ f32x4 mfma(v4u a, v4u b, f32x4 c) {
  return __builtin_amdgcn_mfma_f32_16x16x32_bf16(__builtin_bit_cast(bf16x8, a), __builtin_bit_cast(bf16x8, b), c, 0, 0, 0);
}
__device__ __forceinline__ unsigned fbits(float f) { return __builtin_bit_cast(unsigned, f); }
__device__ __forceinline__ float bitsf(unsigned u) { return __builtin_bit_cast(float, u); }
__device__ __forceinline__ float bflo(unsigned u) { return bitsf(u << 16); }
__device__ __forceinline__ float bfhi(unsigned u) { return bitsf(u & 0xffff0000u); }
__device__ __forceinline__ unsigned cvtpk(float lo, float hi) {
  f32x2 v = {lo, hi};
  return __builtin_bit_cast(unsigned, __builtin_convertvector(v, bf16x2_t));
}
__device__ __forceinline__ float ror8(float x) {
  return bitsf(__builtin_amdgcn_mov_dpp((int)fbits(x), 0x128, 0xf, 0xf, true));
}
__device__ __forceinline__ float swap32_add(float x, float y) {
  auto r = __builtin_amdgcn_permlane32_swap(fbits(x), fbits(y), false, false);
  return bitsf(r[0]) + bitsf(r[1]);
}
__device__ __forceinline__ float swap16_add(float x, float y) {
  auto r = __builtin_amdgcn_permlane16_swap(fbits(x), fbits(y), false, false);
  return bitsf(r[0]) + bitsf(r[1]);
}
__device__ __forceinline__ float swap32_max(float x) {
  auto r = __builtin_amdgcn_permlane32_swap(fbits(x), fbits(x), false, false);
  return fmaxf(bitsf(r[0]), bitsf(r[1]));
}
__device__ __forceinline__ float swap16_max(float x) {
  auto r = __builtin_amdgcn_permlane16_swap(fbits(x), fbits(x), false, false);
  return fmaxf(bitsf(r[0]), bitsf(r[1]));
}
__device__ __forceinline__ float red4_hi(const float (&v)[4]) {
  const float u0 = swap32_add(v[0], v[2]), u1 = swap32_add(v[1], v[3]);
  const float z = swap16_add(u0, u1);
  return z + ror8(z);
}

constexpr unsigned TM = 0x3ffu;
__device__ __forceinline__ v4u tag4u(unsigned a, unsigned b, unsigned c, unsigned d, v4u tg) {
  return v4u{(a & ~TM) | tg[0], (b & ~TM) | tg[1], (c & ~TM) | tg[2], (d & ~TM) | tg[3]};
}
__device__ __forceinline__ v4u tag4(float a, float b, float c, float d, v4u tg) { return tag4u(fbits(a), fbits(b), fbits(c), fbits(d), tg); }
__device__ __forceinline__ bool ok4(v4u x, v4u tg) { return ((((x[0] ^ tg[0]) | (x[1] ^ tg[1])) | ((x[2] ^ tg[2]) | (x[3] ^ tg[3]))) & TM) == 0u; }
__device__ __forceinline__ float val(unsigned x) { return bitsf(x & ~TM); }
__device__ __forceinline__ float val2(unsigned hi, unsigned lo) { return bitsf((hi & ~TM) | ((lo >> 10) & TM)); }
__device__ __forceinline__ void put4(__amdgpu_buffer_rsrc_t rs, int off, v4u v) { __builtin_amdgcn_raw_buffer_store_b128(v, rs, off, 0, AUX_SC1); }
__device__ __forceinline__ v4u get4(__amdgpu_buffer_rsrc_t rs, int off) { return __builtin_amdgcn_raw_buffer_load_b128(rs, off, 0, AUX_SC1); }
template <int N> __device__ __forceinline__ void settle(__amdgpu_buffer_rsrc_t rs, const int (&off)[N], v4u (&x)[N], v4u tg) {
  bool ok = true;
#pragma unroll
  for (int k = 0; k < N; ++k) ok &= ok4(x[k], tg);
  if (__builtin_expect(!__all(ok), 0)) {
#pragma unroll 1
    for (unsigned spins = 0; spins < kSpinCap; ++spins) {
      __builtin_amdgcn_s_sleep(1);
      asm volatile("" ::: "memory");
      if (!ok) {
#pragma unroll
        for (int k = 0; k < N; ++k) x[k] = get4(rs, off[k]);
        ok = true;
#pragma unroll
        for (int k = 0; k < N; ++k) ok &= ok4(x[k], tg);
      }
      if (__all(ok)) break;
    }
  }
}
__device__ __forceinline__ bool last_wave(unsigned* cnt, int lane) {
  unsigned old = 0;
  asm volatile("s_waitcnt lgkmcnt(0)" ::: "memory");
  if (lane == 0) old = __hip_atomic_fetch_add((LAS unsigned*)cnt, 1u, RLX_WG);
  const bool last = __builtin_amdgcn_readfirstlane(old) == NWAVE - 1;
  asm volatile("" ::: "memory");
  return last;
}

struct Lds {
  float red[NWAVE][8][128];
  float qred[NWAVE][128];
  unsigned qs[512];
  float rml[NWAVE][8][2];
  float fin[NWAVE][32];
  float mw[8][NWAVE];
  unsigned short ov[2][32];
  unsigned qready, qcnt, mlcnt;
};

__global__ void __launch_bounds__(NTHR, 2)
gqa_decode_fused(const unsigned short* __restrict__ x, const unsigned short* __restrict__ kc, const unsigned short* __restrict__ vc,
                 const unsigned short* __restrict__ wq, const unsigned short* __restrict__ wo, unsigned short* __restrict__ out,
                 unsigned char* ws) {
  extern __shared__ __attribute__((aligned(16))) unsigned char smem[];
  Lds& L = *(Lds*)smem;
  asm volatile("s_nop 0\n\ts_nop 0");
  const int w = blockIdx.x, t = threadIdx.x, lane = t & 63, wv = __builtin_amdgcn_readfirstlane(t >> 6);
  const unsigned tb = (unsigned)__builtin_amdgcn_s_memrealtime() + T0 + wv * (TAU / 8);
  const int p = w & 7, s = w >> 3, b = p >> 2, kvh = p & 3;
  const int cgq = w >> 2, kq = w & 3;
  const int rb = w >> 1;
  const int cb = (w & 1) ? ECB + wv - 2 : wv - 2;
  const bool oproj = wv >= 2 && cb < ((w & 1) ? 8 : ECB), comb = wv < 2;
  const int a = lane & 15, g = lane >> 4;
  const unsigned char* pc = (const unsigned char*)__builtin_amdgcn_s_getpc();
  const __amdgpu_buffer_rsrc_t rs = __builtin_amdgcn_make_buffer_rsrc((void*)ws, 0, WS_END, 0x00020000);

  int vzero;
  asm volatile("v_mov_b32 %0, 0" : "=v"(vzero));
  const v2u epoch = __builtin_amdgcn_raw_buffer_load_b64(rs, WS_EPOCH + vzero, 0, AUX_SC1);
  const int r0 = 256 * kq + 32 * wv + 4 * (lane >> 3);
  const v2u xa = *(const GAS v2u*)(x + r0), xb = *(const GAS v2u*)(x + 1024 + r0);
  v4u W[4];
#pragma unroll
  for (int i = 0; i < 4; ++i) W[i] = ldnt(wq + (size_t)(r0 + i) * 4096 + 64 * cgq + 8 * (lane & 7));
  if (t == 0) { L.qready = 0u; L.qcnt = 0u; L.mlcnt = 0u; }
  PIN();
  __syncthreads();
  PIN();
  const size_t base = ((size_t)p * SK + (size_t)s * 1024 + wv * 16) * (DH * 2);
  const int kap = a >> 1;
  const unsigned char* kb = (const unsigned char*)kc + base + (4 * (kap & 1) + (kap >> 1)) * 256 + (2 * g + (a & 1)) * 16;
  const unsigned char* vb = (const unsigned char*)vc + base + lane * 16;
  v4u R[4][8];
#define GOFF(n) (((n) >> 1) * 32768 + ((n) & 1) * 2048)
#define ISSUE_K(n) do { R[(n) >> 2][2 * ((n) & 3)] = ldnt(kb + GOFF(n)); R[(n) >> 2][2 * ((n) & 3) + 1] = ldnt(kb + GOFF(n) + 128); PIN(); } while (0)
#define ISSUE_V(n) do { R[(n) >> 2][2 * ((n) & 3)] = ldnt(vb + GOFF(n)); R[(n) >> 2][2 * ((n) & 3) + 1] = ldnt(vb + GOFF(n) + 1024); PIN(); } while (0)
#pragma unroll
  for (int n = 0; n < NPRE; ++n) ISSUE_K(n);
  unsigned warm0 = 0, warm1 = 0;
  if (w < WARM_WGS && wv == NWAVE - 1) {
    warm0 = *(const GAS unsigned*)(pc + lane * 128);
    if (lane < (CODE_BYTES - 8192) / 128) warm1 = *(const GAS unsigned*)(pc + 8192 + lane * 128);
  }
  PIN();

  {
    float acc[2][8];
#pragma unroll
    for (int j = 0; j < 8; ++j) { acc[0][j] = 0.f; acc[1][j] = 0.f; }
#pragma unroll
    for (int i = 0; i < 4; ++i) {
      const float x0 = (i & 1) ? bfhi(xa[i >> 1]) : bflo(xa[i >> 1]);
      const float x1 = (i & 1) ? bfhi(xb[i >> 1]) : bflo(xb[i >> 1]);
#pragma unroll
      for (int u = 0; u < 4; ++u) {
        const float wa = bflo(W[i][u]), wb = bfhi(W[i][u]);
        acc[0][2 * u] += x0 * wa; acc[0][2 * u + 1] += x0 * wb;
        acc[1][2 * u] += x1 * wa; acc[1][2 * u + 1] += x1 * wb;
      }
    }
    float v8[8], v4[4], v2[2];
#pragma unroll
    for (int j = 0; j < 8; ++j) v8[j] = swap32_add(acc[0][j], acc[1][j]);
#pragma unroll
    for (int j = 0; j < 4; ++j) v4[j] = swap16_add(v8[j], v8[j + 4]);
    const bool b3 = lane & 8;
#pragma unroll
    for (int j = 0; j < 2; ++j) {
      const float keep = b3 ? v4[j + 2] : v4[j], send = b3 ? v4[j] : v4[j + 2];
      v2[j] = keep + ror8(send);
    }
    const int col = 8 * (lane & 7) + 4 * ((lane >> 4) & 1) + 2 * ((lane >> 3) & 1);
    *(f32x2*)&L.qred[wv][(lane >> 5) * 64 + col] = f32x2{v2[0], v2[1]};
  }
  const v4u tg = {((epoch[0] & TM) * 5u + 0x147u) & TM, (((epoch[0] >> 10) & TM) * 5u + 0x2b5u) & TM,
                  ((epoch[1] & TM) * 5u + 0x09du) & TM, (((epoch[1] >> 10) & TM) * 5u + 0x3a9u) & TM};
  if (last_wave(&L.qcnt, lane)) {
    if (lane < 32) {
      f32x4 sum = *(const f32x4*)&L.qred[0][4 * lane];
#pragma unroll
      for (int ww = 1; ww < NWAVE; ++ww) sum += *(const f32x4*)&L.qred[ww][4 * lane];
      put4(rs, WS_QP + w * 512 + lane * 16, tag4(sum[0], sum[1], sum[2], sum[3], tg));
    }
  }
  PIN();
  const int oq = WS_QP + ((16 * kvh + 2 * wv + (lane >> 5)) * 4 + 2 * (g & 1)) * 512 + b * 256 + a * 16;
  const int offq[2] = {oq, oq + 512};
  v4u xq[2];
#pragma unroll
  for (int n = NPRE; n < 16; ++n) {
    PACE(n); ISSUE_K(n);
    if (n == QPOLL_AT) { xq[0] = get4(rs, offq[0]); xq[1] = get4(rs, offq[1]); PIN(); }
    if (n == QPOLL_AT + 1) {
      settle<2>(rs, offq, xq, tg);
      float q4[4];
#pragma unroll
      for (int e = 0; e < 4; ++e) { const float v = val(xq[0][e]) + val(xq[1][e]); q4[e] = swap16_add(v, v); }
      if (!(g & 1)) *(v2u*)&L.qs[wv * 64 + (lane >> 5) * 32 + 2 * a] = v2u{cvtpk(q4[0], q4[1]), cvtpk(q4[2], q4[3])};
      asm volatile("s_waitcnt lgkmcnt(0)" ::: "memory");
      if (lane == 0) __hip_atomic_fetch_add((LAS unsigned*)&L.qready, 1u, RLX_WG);
      PIN();
    }
  }
  asm volatile("" :: "v"(warm0), "v"(warm1));
  while (__hip_atomic_load((LAS unsigned*)&L.qready, RLX_WG) != NWAVE) __builtin_amdgcn_s_sleep(1);
  PIN();
  v4u qf[2];
#pragma unroll
  for (int dh = 0; dh < 2; ++dh) qf[dh] = *(const v4u*)&L.qs[(a & 7) * 64 + 32 * dh + 8 * g + 4 * (a >> 3)];

  float S[4][8];
  const bool cp = a & 8;
#pragma unroll
  for (int i = 0; i < 4; ++i) {
#pragma unroll
    for (int gm = 0; gm < 4; ++gm) {
      f32x4 c = {0.f, 0.f, 0.f, 0.f};
      c = mfma(R[i][2 * gm], qf[0], c);
      c = mfma(R[i][2 * gm + 1], qf[1], c);
      const float u0 = cp ? c[1] : c[0], u1 = cp ? c[3] : c[2];
      S[i][2 * gm] = u0 + ror8(u0);
      S[i][2 * gm + 1] = u1 + ror8(u1);
    }
    PIN();
    if (i == 0) { ISSUE_V(0); ISSUE_V(1); }
    if (i == 3) { ISSUE_V(2); ISSUE_V(3); }
  }
  float m = S[0][0];
#pragma unroll
  for (int i = 0; i < 4; ++i)
#pragma unroll
    for (int j = 0; j < 8; ++j) m = fmaxf(m, S[i][j]);
  m = swap16_max(m);
  m = swap32_max(m);
  const float mc = m * C2;
  float l = 0.f;
  v4u P[4];
#pragma unroll
  for (int i = 0; i < 4; ++i) {
    float e[8];
#pragma unroll
    for (int j = 0; j < 8; ++j) { e[j] = __builtin_amdgcn_exp2f(S[i][j] * C2 - mc); l += e[j]; }
    P[i] = v4u{cvtpk(e[0], e[1]), cvtpk(e[2], e[3]), cvtpk(e[4], e[5]), cvtpk(e[6], e[7])};
  }
  l = swap16_add(l, l);
  l = swap32_add(l, l);
  if (lane < 8) { L.rml[wv][lane][0] = mc; L.rml[wv][lane][1] = l; }
  if (last_wave(&L.mlcnt, lane)) {
    if (lane < 8) {
      float M = L.rml[0][lane][0];
#pragma unroll
      for (int ww = 1; ww < NWAVE; ++ww) M = fmaxf(M, L.rml[ww][lane][0]);
      float Ls = 0.f;
#pragma unroll
      for (int ww = 0; ww < NWAVE; ++ww) {
        const float f = __builtin_amdgcn_exp2f(L.rml[ww][lane][0] - M);
        L.mw[lane][ww] = f;
        Ls += f * L.rml[ww][lane][1];
      }
      put4(rs, WS_PO + w * PO_STR + 4096 + lane * 16, tag4u(fbits(M), fbits(Ls), fbits(M) << 10, fbits(Ls) << 10, tg));
    }
  }
  PIN();
  f32x4 O[8];
#pragma unroll
  for (int e = 0; e < 8; ++e) O[e] = f32x4{0.f, 0.f, 0.f, 0.f};
  const int po0 = WS_PO + ((lane >> 3) * 8 + (wv & 1) * 4 + (rb >> 5)) * PO_STR;
  const int om = po0 + 4096 + ((rb >> 2) & 7) * 16;
  const int offm[4] = {om, om + 64 * PO_STR, om + 128 * PO_STR, om + 192 * PO_STR};
  v4u ML[4] = {};
#pragma unroll
  for (int i = 0; i < 4; ++i) {
    if (i == 0) { PACE(16 + 4); ISSUE_V(4); PACE(16 + 5); ISSUE_V(5); PACE(16 + 6); ISSUE_V(6); }
    if (i == 1) { PACE(16 + 7); ISSUE_V(7); PACE(16 + 8); ISSUE_V(8); PACE(16 + 9); ISSUE_V(9); PACE(16 + 10); ISSUE_V(10); }
    if (i == 2) {
      PACE(16 + 11); ISSUE_V(11); PACE(16 + 12); ISSUE_V(12);
      if (comb) {
#pragma unroll
        for (int k = 0; k < 4; ++k) ML[k] = get4(rs, offm[k]);
      }
      PIN();
      PACE(16 + 13); ISSUE_V(13); PACE(16 + 14); ISSUE_V(14); PACE(16 + 15); ISSUE_V(15);
    }
#pragma unroll
    for (int e = 0; e < 8; ++e) {
      const unsigned sel = (e & 1) ? 0x07060302u : 0x05040100u;
      v4u bv;
#pragma unroll
      for (int u = 0; u < 4; ++u) bv[u] = __builtin_amdgcn_perm(R[i][2 * u + 1][e >> 1], R[i][2 * u][e >> 1], sel);
      O[e] = mfma(P[i], bv, O[e]);
    }
    PIN();
  }
  v4u WO[8];
  if (oproj) {
#pragma unroll
    for (int j = 0; j < 8; ++j) WO[j] = ldnt(wo + (size_t)(32 * rb + 8 * g + j) * 1024 + 128 * cb + 8 * a);
  }
  PIN();
  if (g < 2) {
#pragma unroll
    for (int r = 0; r < 4; ++r) {
      *(f32x4*)&L.red[wv][4 * g + r][8 * a] = f32x4{O[0][r], O[1][r], O[2][r], O[3][r]};
      *(f32x4*)&L.red[wv][4 * g + r][8 * a + 4] = f32x4{O[4][r], O[5][r], O[6][r], O[7][r]};
    }
  }
  __syncthreads();
  if (lane < 32) {
    const f32x4 f0 = *(const f32x4*)&L.mw[wv][0], f1 = *(const f32x4*)&L.mw[wv][4];
    f32x4 pp[NWAVE];
#pragma unroll
    for (int ww = 0; ww < NWAVE; ++ww) pp[ww] = *(const f32x4*)&L.red[ww][wv][4 * lane];
    __builtin_amdgcn_sched_barrier(0);
    const f32x4 o = ((f0[0] * pp[0] + f0[1] * pp[1]) + (f0[2] * pp[2] + f0[3] * pp[3])) + ((f1[0] * pp[4] + f1[1] * pp[5]) + (f1[2] * pp[6] + f1[3] * pp[7]));
    put4(rs, WS_PO + w * PO_STR + wv * 512 + lane * 16, tag4(o[0], o[1], o[2], o[3], tg));
  }
  PIN();
  v4u BW[8];
  if (comb) {
    const int oo = po0 + ((rb >> 2) & 7) * 512 + (rb & 3) * 128 + (lane & 7) * 16;
    const int off[4] = {oo, oo + 64 * PO_STR, oo + 128 * PO_STR, oo + 192 * PO_STR};
    v4u xx[4];
    __builtin_amdgcn_s_sleep(NAP1);
#pragma unroll
    for (int k = 0; k < 4; ++k) xx[k] = get4(rs, off[k]);
    PIN();
    float wt[4];
    settle<4>(rs, offm, ML, tg);
    float ms[4];
#pragma unroll
    for (int k = 0; k < 4; ++k) ms[k] = val2(ML[k][0], ML[k][2]);
    float M = fmaxf(fmaxf(ms[0], ms[1]), fmaxf(ms[2], ms[3]));
    M = fmaxf(M, ror8(M));
    M = swap16_max(M); M = swap32_max(M);
    float Ls = 0.f;
#pragma unroll
    for (int k = 0; k < 4; ++k) { wt[k] = __builtin_amdgcn_exp2f(ms[k] - M); Ls += wt[k] * val2(ML[k][1], ML[k][3]); }
    Ls = swap32_add(Ls, Ls); Ls = swap16_add(Ls, Ls); Ls += ror8(Ls);
    const float inv = 1.0f / Ls;
#pragma unroll
    for (int k = 0; k < 4; ++k) { wt[k] *= inv; asm volatile("" : "+v"(wt[k])); }
    PIN();
    settle<4>(rs, off, xx, tg);
    float o[4];
#pragma unroll
    for (int e = 0; e < 4; ++e) o[e] = (wt[0] * val(xx[0][e]) + wt[1] * val(xx[1][e])) + (wt[2] * val(xx[2][e]) + wt[3] * val(xx[3][e]));
    const unsigned pk = cvtpk(red4_hi(o), 0.f);
    if ((lane & 8) == 0) L.ov[wv][4 * (lane & 7) + 2 * (lane >> 5) + ((lane >> 4) & 1)] = (unsigned short)pk;
  } else if (oproj) {
#pragma unroll
    for (int e = 0; e < 8; ++e)
#pragma unroll
      for (int u = 0; u < 4; ++u) BW[e][u] = __builtin_amdgcn_perm(WO[2 * u + 1][e >> 1], WO[2 * u][e >> 1], (e & 1) ? 0x07060302u : 0x05040100u);
#pragma unroll
    for (int e = 0; e < 8; ++e) asm volatile("" : "+v"(BW[e]));
  }
  PIN();
  __syncthreads();
  if (oproj) {
    const v4u ao = *(const v4u*)&L.ov[(a >> 2) & 1][8 * g];
    f32x4 d[8];
#pragma unroll
    for (int e = 0; e < 8; ++e) d[e] = mfma(ao, BW[e], f32x4{0.f, 0.f, 0.f, 0.f});
    const bool hi = g >> 1;
    put4(rs, WS_P2 + rb * 8192 + (g & 1) * 4096 + cb * 512 + a * 32 + (g >> 1) * 16,
         tag4(hi ? d[4][0] : d[0][0], hi ? d[5][0] : d[1][0], hi ? d[6][0] : d[2][0], hi ? d[7][0] : d[3][0], tg));
  }
  PIN();
  if ((w & 1) && w < 128) {
    const int o0 = WS_P2 + (16 * wv + (lane >> 3)) * 8192 + rb * 128 + (lane & 7) * 16;
    const int of2[2] = {o0, o0 + 8 * 8192};
    __builtin_amdgcn_s_sleep(NAP2);
    v4u yy[2] = {get4(rs, of2[0]), get4(rs, of2[1])};
    settle<2>(rs, of2, yy, tg);
    float v[4];
#pragma unroll
    for (int e = 0; e < 4; ++e) v[e] = val(yy[0][e]) + val(yy[1][e]);
    const float z = red4_hi(v);
    if ((lane & 8) == 0) L.fin[wv][4 * (lane & 7) + 2 * (lane >> 5) + ((lane >> 4) & 1)] = z;
    __syncthreads();
    if (t < 16) {
      f32x2 sm = *(const f32x2*)&L.fin[0][2 * t];
#pragma unroll
      for (int ww = 1; ww < NWAVE; ++ww) sm += *(const f32x2*)&L.fin[ww][2 * t];
      ((unsigned*)out)[rb * 16 + t] = cvtpk(sm[0], sm[1]);
    }
    if (w == 1 && t == 0) __builtin_amdgcn_raw_buffer_store_b64(v2u{tg[0] | (tg[1] << 10), tg[2] | (tg[3] << 10)}, rs, WS_EPOCH, 0, AUX_SC1);
  }
}

extern "C" void kernel_launch(void* const* d_in, const int* in_sizes, int n_in, void* d_out, int out_size, void* d_ws,
                              size_t ws_size, hipStream_t stream) {
  constexpr int kLds = 96 * 1024;
  static_assert(sizeof(Lds) <= kLds, "LDS");
  (void)hipFuncSetAttribute((const void*)gqa_decode_fused, hipFuncAttributeMaxDynamicSharedMemorySize, kLds);
  gqa_decode_fused<<<NWG, NTHR, kLds, stream>>>((const unsigned short*)d_in[0], (const unsigned short*)d_in[1],
                                                (const unsigned short*)d_in[2], (const unsigned short*)d_in[3],
                                                (const unsigned short*)d_in[4], (unsigned short*)d_out, (unsigned char*)d_ws);
}
```

```cpp
#include <hip/hip_runtime.h>
#include <hip/hip_bf16.h>
#include <stdint.h>

typedef unsigned v4u __attribute__((ext_vector_type(4)));
typedef unsigned v2u __attribute__((ext_vector_type(2)));
typedef float f32x4 __attribute__((ext_vector_type(4)));
typedef float f32x2 __attribute__((ext_vector_type(2)));
typedef short bf16x8 __attribute__((ext_vector_type(8)));
typedef __bf16 bf16x2_t __attribute__((ext_vector_type(2)));
#define GAS __attribute__((address_space(1)))
#define LAS __attribute__((address_space(3)))
#define RLX_WG __ATOMIC_RELAXED, __HIP_MEMORY_SCOPE_WORKGROUP

constexpr int SK = 32768, DH = 128;
constexpr int NWG = 256, NTHR = 512, NWAVE = 8;
constexpr float C2 = 0.08838834764831845f * 1.4426950408889634f;
constexpr unsigned kSpinCap = 1u << 17;
constexpr int NPRE = 4;
constexpr int QPOLL_AT = 10;
constexpr int ECB = 6;
constexpr int AUX_SC1 = 16;

#define DUE(n) ((n) < 16 ? 190 + ((n) - 4) * 66 : 1102 + ((n) - 20) * 67)
#define PACE(n) do { asm volatile("s_waitcnt vmcnt(2)" ::: "memory"); \
    while ((int)((unsigned)__builtin_amdgcn_s_memrealtime() - (tb + (unsigned)DUE(n))) < 0) __builtin_amdgcn_s_sleep(1); } while (0)
constexpr int NAP1 = 26, NAP2 = 24;
#define PIN() do { asm volatile("" ::: "memory"); __builtin_amdgcn_sched_barrier(0); } while (0)

constexpr int WS_EPOCH = 0;
constexpr int WS_QP = 4096;
constexpr int WS_PO = WS_QP + 256 * 512;
constexpr int PO_STR = 4096 + 128;
constexpr int WS_P2 = WS_PO + 256 * PO_STR;
constexpr int WS_END = WS_P2 + 128 * 8192;

__device__ __forceinline__ v4u ldnt(const void* p) { return __builtin_nontemporal_load((const GAS v4u*)p); }
__device__ __forceinline__ f32x4 mfma(v4u a, v4u b, f32x4 c) {
  return __builtin_amdgcn_mfma_f32_16x16x32_bf16(__builtin_bit_cast(bf16x8, a), __builtin_bit_cast(bf16x8, b), c, 0, 0, 0);
}
__device__ __forceinline__ unsigned fbits(float f) { return __builtin_bit_cast(unsigned, f); }
__device__ __forceinline__ float bitsf(unsigned u) { return __builtin_bit_cast(float, u); }
__device__ __forceinline__ float bflo(unsigned u) { return bitsf(u << 16); }
__device__ __forceinline__ float bfhi(unsigned u) { return bitsf(u & 0xffff0000u); }
__device__ __forceinline__ unsigned cvtpk(float lo, float hi) {
  f32x2 v = {lo, hi};
  return __builtin_bit_cast(unsigned, __builtin_convertvector(v, bf16x2_t));
}
__device__ __forceinline__ float ror8(float x) {
  return bitsf(__builtin_amdgcn_mov_dpp((int)fbits(x), 0x128, 0xf, 0xf, true));
}
__device__ __forceinline__ float swap32_add(float x, float y) {
  auto r = __builtin_amdgcn_permlane32_swap(fbits(x), fbits(y), false, false);
  return bitsf(r[0]) + bitsf(r[1]);
}
__device__ __forceinline__ float swap16_add(float x, float y) {
  auto r = __builtin_amdgcn_permlane16_swap(fbits(x), fbits(y), false, false);
  return bitsf(r[0]) + bitsf(r[1]);
}
__device__ __forceinline__ float swap32_max(float x) {
  auto r = __builtin_amdgcn_permlane32_swap(fbits(x), fbits(x), false, false);
  return fmaxf(bitsf(r[0]), bitsf(r[1]));
}
__device__ __forceinline__ float swap16_max(float x) {
  auto r = __builtin_amdgcn_permlane16_swap(fbits(x), fbits(x), false, false);
  return fmaxf(bitsf(r[0]), bitsf(r[1]));
}
__device__ __forceinline__ float red4_hi(const float (&v)[4]) {
  const float u0 = swap32_add(v[0], v[2]), u1 = swap32_add(v[1], v[3]);
  const float z = swap16_add(u0, u1);
  return z + ror8(z);
}

constexpr unsigned TM = 0x3ffu;
__device__ __forceinline__ v4u tag4u(unsigned a, unsigned b, unsigned c, unsigned d, v4u tg) {
  return v4u{(a & ~TM) | tg[0], (b & ~TM) | tg[1], (c & ~TM) | tg[2], (d & ~TM) | tg[3]};
}
__device__ __forceinline__ v4u tag4(float a, float b, float c, float d, v4u tg) { return tag4u(fbits(a), fbits(b), fbits(c), fbits(d), tg); }
__device__ __forceinline__ bool ok4(v4u x, v4u tg) { return ((((x[0] ^ tg[0]) | (x[1] ^ tg[1])) | ((x[2] ^ tg[2]) | (x[3] ^ tg[3]))) & TM) == 0u; }
__device__ __forceinline__ float val(unsigned x) { return bitsf(x & ~TM); }
__device__ __forceinline__ float val2(unsigned hi, unsigned lo) { return bitsf((hi & ~TM) | ((lo >> 10) & TM)); }
__device__ __forceinline__ void put4(__amdgpu_buffer_rsrc_t rs, int off, v4u v) { __builtin_amdgcn_raw_buffer_store_b128(v, rs, off, 0, AUX_SC1); }
__device__ __forceinline__ v4u get4(__amdgpu_buffer_rsrc_t rs, int off) { return __builtin_amdgcn_raw_buffer_load_b128(rs, off, 0, AUX_SC1); }
template <int N> __device__ __forceinline__ void settle(__amdgpu_buffer_rsrc_t rs, const int (&off)[N], v4u (&x)[N], v4u tg) {
  bool ok = true;
#pragma unroll
  for (int k = 0; k < N; ++k) ok &= ok4(x[k], tg);
  if (__builtin_expect(!__all(ok), 0)) {
#pragma unroll 1
    for (unsigned spins = 0; spins < kSpinCap; ++spins) {
      __builtin_amdgcn_s_sleep(1);
      asm volatile("" ::: "memory");
      if (!ok) {
#pragma unroll
        for (int k = 0; k < N; ++k) x[k] = get4(rs, off[k]);
        ok = true;
#pragma unroll
        for (int k = 0; k < N; ++k) ok &= ok4(x[k], tg);
      }
      if (__all(ok)) break;
    }
  }
}
__device__ __forceinline__ bool last_wave(unsigned* cnt, int lane) {
  unsigned old = 0;
  asm volatile("s_waitcnt lgkmcnt(0)" ::: "memory");
  if (lane == 0) old = __hip_atomic_fetch_add((LAS unsigned*)cnt, 1u, RLX_WG);
  const bool last = __builtin_amdgcn_readfirstlane(old) == NWAVE - 1;
  asm volatile("" ::: "memory");
  return last;
}

struct Lds {
  float red[NWAVE][8][128];
  float qred[NWAVE][128];
  unsigned qs[512];
  float rml[NWAVE][8][2];
  float fin[NWAVE][32];
  float mw[8][NWAVE];
  unsigned short ov[2][32];
  unsigned qready, qcnt, mlcnt;
};

__global__ void __launch_bounds__(NTHR, 2)
gqa_decode_fused(const unsigned short* __restrict__ x, const unsigned short* __restrict__ kc, const unsigned short* __restrict__ vc,
                 const unsigned short* __restrict__ wq, const unsigned short* __restrict__ wo, unsigned short* __restrict__ out,
                 unsigned char* ws) {
  extern __shared__ __attribute__((aligned(16))) unsigned char smem[];
  Lds& L = *(Lds*)smem;
  const int w = blockIdx.x, t = threadIdx.x, lane = t & 63, wv = __builtin_amdgcn_readfirstlane(t >> 6);
  const unsigned tb = (unsigned)__builtin_amdgcn_s_memrealtime() + wv * 8;
  const int p = w & 7, s = w >> 3, b = p >> 2, kvh = p & 3;
  const int cgq = w >> 2, kq = w & 3;
  const int rb = w >> 1;
  const int cb = (w & 1) ? ECB + wv - 2 : wv - 2;
  const bool oproj = wv >= 2 && cb < ((w & 1) ? 8 : ECB), comb = wv < 2;
  const int a = lane & 15, g = lane >> 4;
  const __amdgpu_buffer_rsrc_t rs = __builtin_amdgcn_make_buffer_rsrc((void*)ws, 0, WS_END, 0x00020000);

  int vzero;
  asm volatile("v_mov_b32 %0, 0" : "=v"(vzero));
  const v2u epoch = __builtin_amdgcn_raw_buffer_load_b64(rs, WS_EPOCH + vzero, 0, AUX_SC1);
  const int r0 = 256 * kq + 32 * wv + 4 * (lane >> 3);
  const v2u xa = *(const GAS v2u*)(x + r0), xb = *(const GAS v2u*)(x + 1024 + r0);
  v4u W[4];
#pragma unroll
  for (int i = 0; i < 4; ++i) W[i] = ldnt(wq + (size_t)(r0 + i) * 4096 + 64 * cgq + 8 * (lane & 7));
  if (t == 0) { L.qready = 0u; L.qcnt = 0u; L.mlcnt = 0u; }
  PIN();
  __syncthreads();
  PIN();
  const size_t base = ((size_t)p * SK + (size_t)s * 1024 + wv * 16) * (DH * 2);
  const int kap = a >> 1;
  const unsigned char* kb = (const unsigned char*)kc + base + (4 * (kap & 1) + (kap >> 1)) * 256 + (2 * g + (a & 1)) * 16;
  const unsigned char* vb = (const unsigned char*)vc + base + lane * 16;
  v4u R[4][8];
#define GOFF(n) (((n) >> 1) * 32768 + ((n) & 1) * 2048)
#define ISSUE_K(n) do { R[(n) >> 2][2 * ((n) & 3)] = ldnt(kb + GOFF(n)); R[(n) >> 2][2 * ((n) & 3) + 1] = ldnt(kb + GOFF(n) + 128); PIN(); } while (0)
#define ISSUE_V(n) do { R[(n) >> 2][2 * ((n) & 3)] = ldnt(vb + GOFF(n)); R[(n) >> 2][2 * ((n) & 3) + 1] = ldnt(vb + GOFF(n) + 1024); PIN(); } while (0)
#pragma unroll
  for (int n = 0; n < NPRE; ++n) ISSUE_K(n);

  {
    float acc[2][8];
#pragma unroll
    for (int j = 0; j < 8; ++j) { acc[0][j] = 0.f; acc[1][j] = 0.f; }
#pragma unroll
    for (int i = 0; i < 4; ++i) {
      const float x0 = (i & 1) ? bfhi(xa[i >> 1]) : bflo(xa[i >> 1]);
      const float x1 = (i & 1) ? bfhi(xb[i >> 1]) : bflo(xb[i >> 1]);
#pragma unroll
      for (int u = 0; u < 4; ++u) {
        const float wa = bflo(W[i][u]), wb = bfhi(W[i][u]);
        acc[0][2 * u] += x0 * wa; acc[0][2 * u + 1] += x0 * wb;
        acc[1][2 * u] += x1 * wa; acc[1][2 * u + 1] += x1 * wb;
      }
    }
    float v8[8], v4[4], v2[2];
#pragma unroll
    for (int j = 0; j < 8; ++j) v8[j] = swap32_add(acc[0][j], acc[1][j]);
#pragma unroll
    for (int j = 0; j < 4; ++j) v4[j] = swap16_add(v8[j], v8[j + 4]);
    const bool b3 = lane & 8;
#pragma unroll
    for (int j = 0; j < 2; ++j) {
      const float keep = b3 ? v4[j + 2] : v4[j], send = b3 ? v4[j] : v4[j + 2];
      v2[j] = keep + ror8(send);
    }
    const int col = 8 * (lane & 7) + 4 * ((lane >> 4) & 1) + 2 * ((lane >> 3) & 1);
    *(f32x2*)&L.qred[wv][(lane >> 5) * 64 + col] = f32x2{v2[0], v2[1]};
  }
  const v4u tg = {((epoch[0] & TM) * 5u + 0x147u) & TM, (((epoch[0] >> 10) & TM) * 5u + 0x2b5u) & TM,
                  ((epoch[1] & TM) * 5u + 0x09du) & TM, (((epoch[1] >> 10) & TM) * 5u + 0x3a9u) & TM};
  if (last_wave(&L.qcnt, lane)) {
    if (lane < 32) {
      f32x4 sum = *(const f32x4*)&L.qred[0][4 * lane];
#pragma unroll
      for (int ww = 1; ww < NWAVE; ++ww) sum += *(const f32x4*)&L.qred[ww][4 * lane];
      put4(rs, WS_QP + w * 512 + lane * 16, tag4(sum[0], sum[1], sum[2], sum[3], tg));
    }
  }
  PIN();
  const int oq = WS_QP + ((16 * kvh + 2 * wv + (lane >> 5)) * 4 + 2 * (g & 1)) * 512 + b * 256 + a * 16;
  const int offq[2] = {oq, oq + 512};
  v4u xq[2];
#pragma unroll
  for (int n = NPRE; n < 16; ++n) {
    PACE(n); ISSUE_K(n);
    if (n == QPOLL_AT) { xq[0] = get4(rs, offq[0]); xq[1] = get4(rs, offq[1]); PIN(); }
    if (n == QPOLL_AT + 1) {
      settle<2>(rs, offq, xq, tg);
      float q4[4];
#pragma unroll
      for (int e = 0; e < 4; ++e) { const float v = val(xq[0][e]) + val(xq[1][e]); q4[e] = swap16_add(v, v); }
      if (!(g & 1)) *(v2u*)&L.qs[wv * 64 + (lane >> 5) * 32 + 2 * a] = v2u{cvtpk(q4[0], q4[1]), cvtpk(q4[2], q4[3])};
      asm volatile("s_waitcnt lgkmcnt(0)" ::: "memory");
      if (lane == 0) __hip_atomic_fetch_add((LAS unsigned*)&L.qready, 1u, RLX_WG);
      PIN();
    }
  }
  while (__hip_atomic_load((LAS unsigned*)&L.qready, RLX_WG) != NWAVE) __builtin_amdgcn_s_sleep(1);
  PIN();
  v4u qf[2];
#pragma unroll
  for (int dh = 0; dh < 2; ++dh) qf[dh] = *(const v4u*)&L.qs[(a & 7) * 64 + 32 * dh + 8 * g + 4 * (a >> 3)];

  float S[4][8];
  const bool cp = a & 8;
#pragma unroll
  for (int i = 0; i < 4; ++i) {
#pragma unroll
    for (int gm = 0; gm < 4; ++gm) {
      f32x4 c = {0.f, 0.f, 0.f, 0.f};
      c = mfma(R[i][2 * gm], qf[0], c);
      c = mfma(R[i][2 * gm + 1], qf[1], c);
      const float u0 = cp ? c[1] : c[0], u1 = cp ? c[3] : c[2];
      S[i][2 * gm] = u0 + ror8(u0);
      S[i][2 * gm + 1] = u1 + ror8(u1);
    }
    PIN();
    if (i == 0) { ISSUE_V(0); ISSUE_V(1); }
    if (i == 3) { ISSUE_V(2); ISSUE_V(3); }
  }
  float m = S[0][0];
#pragma unroll
  for (int i = 0; i < 4; ++i)
#pragma unroll
    for (int j = 0; j < 8; ++j) m = fmaxf(m, S[i][j]);
  m = swap16_max(m);
  m = swap32_max(m);
  const float mc = m * C2;
  float l = 0.f;
  v4u P[4];
#pragma unroll
  for (int i = 0; i < 4; ++i) {
    float e[8];
#pragma unroll
    for (int j = 0; j < 8; ++j) { e[j] = __builtin_amdgcn_exp2f(S[i][j] * C2 - mc); l += e[j]; }
    P[i] = v4u{cvtpk(e[0], e[1]), cvtpk(e[2], e[3]), cvtpk(e[4], e[5]), cvtpk(e[6], e[7])};
  }
  l = swap16_add(l, l);
  l = swap32_add(l, l);
  if (lane < 8) { L.rml[wv][lane][0] = mc; L.rml[wv][lane][1] = l; }
  if (last_wave(&L.mlcnt, lane)) {
    if (lane < 8) {
      float M = L.rml[0][lane][0];
#pragma unroll
      for (int ww = 1; ww < NWAVE; ++ww) M = fmaxf(M, L.rml[ww][lane][0]);
      float Ls = 0.f;
#pragma unroll
      for (int ww = 0; ww < NWAVE; ++ww) {
        const float f = __builtin_amdgcn_exp2f(L.rml[ww][lane][0] - M);
        L.mw[lane][ww] = f;
        Ls += f * L.rml[ww][lane][1];
      }
      put4(rs, WS_PO + w * PO_STR + 4096 + lane * 16, tag4u(fbits(M), fbits(Ls), fbits(M) << 10, fbits(Ls) << 10, tg));
    }
  }
  PIN();
  f32x4 O[8];
#pragma unroll
  for (int e = 0; e < 8; ++e) O[e] = f32x4{0.f, 0.f, 0.f, 0.f};
  const int po0 = WS_PO + ((lane >> 3) * 8 + (wv & 1) * 4 + (rb >> 5)) * PO_STR;
  const int om = po0 + 4096 + ((rb >> 2) & 7) * 16;
  const int offm[4] = {om, om + 64 * PO_STR, om + 128 * PO_STR, om + 192 * PO_STR};
  v4u ML[4] = {};
#pragma unroll
  for (int i = 0; i < 4; ++i) {
    if (i == 0) { PACE(16 + 4); ISSUE_V(4); PACE(16 + 5); ISSUE_V(5); PACE(16 + 6); ISSUE_V(6); }
    if (i == 1) { PACE(16 + 7); ISSUE_V(7); PACE(16 + 8); ISSUE_V(8); PACE(16 + 9); ISSUE_V(9); PACE(16 + 10); ISSUE_V(10); }
    if (i == 2) {
      PACE(16 + 11); ISSUE_V(11); PACE(16 + 12); ISSUE_V(12);
      if (comb) {
#pragma unroll
        for (int k = 0; k < 4; ++k) ML[k] = get4(rs, offm[k]);
      }
      PIN();
      PACE(16 + 13); ISSUE_V(13); PACE(16 + 14); ISSUE_V(14); PACE(16 + 15); ISSUE_V(15);
    }
#pragma unroll
    for (int e = 0; e < 8; ++e) {
      const unsigned sel = (e & 1) ? 0x07060302u : 0x05040100u;
      v4u bv;
#pragma unroll
      for (int u = 0; u < 4; ++u) bv[u] = __builtin_amdgcn_perm(R[i][2 * u + 1][e >> 1], R[i][2 * u][e >> 1], sel);
      O[e] = mfma(P[i], bv, O[e]);
    }
    PIN();
  }
  v4u WO[8];
  if (oproj) {
#pragma unroll
    for (int j = 0; j < 8; ++j) WO[j] = ldnt(wo + (size_t)(32 * rb + 8 * g + j) * 1024 + 128 * cb + 8 * a);
  }
  PIN();
  if (g < 2) {
#pragma unroll
    for (int r = 0; r < 4; ++r) {
      *(f32x4*)&L.red[wv][4 * g + r][8 * a] = f32x4{O[0][r], O[1][r], O[2][r], O[3][r]};
      *(f32x4*)&L.red[wv][4 * g + r][8 * a + 4] = f32x4{O[4][r], O[5][r], O[6][r], O[7][r]};
    }
  }
  __syncthreads();
  if (lane < 32) {
    const f32x4 f0 = *(const f32x4*)&L.mw[wv][0], f1 = *(const f32x4*)&L.mw[wv][4];
    f32x4 pp[NWAVE];
#pragma unroll
    for (int ww = 0; ww < NWAVE; ++ww) pp[ww] = *(const f32x4*)&L.red[ww][wv][4 * lane];
    __builtin_amdgcn_sched_barrier(0);
    const f32x4 o = ((f0[0] * pp[0] + f0[1] * pp[1]) + (f0[2] * pp[2] + f0[3] * pp[3])) + ((f1[0] * pp[4] + f1[1] * pp[5]) + (f1[2] * pp[6] + f1[3] * pp[7]));
    put4(rs, WS_PO + w * PO_STR + wv * 512 + lane * 16, tag4(o[0], o[1], o[2], o[3], tg));
  }
  PIN();
  v4u BW[8];
  if (comb) {
    const int oo = po0 + ((rb >> 2) & 7) * 512 + (rb & 3) * 128 + (lane & 7) * 16;
    const int off[4] = {oo, oo + 64 * PO_STR, oo + 128 * PO_STR, oo + 192 * PO_STR};
    v4u xx[4];
    __builtin_amdgcn_s_sleep(NAP1);
#pragma unroll
    for (int k = 0; k < 4; ++k) xx[k] = get4(rs, off[k]);
    PIN();
    float wt[4];
    settle<4>(rs, offm, ML, tg);
    float ms[4];
#pragma unroll
    for (int k = 0; k < 4; ++k) ms[k] = val2(ML[k][0], ML[k][2]);
    float M = fmaxf(fmaxf(ms[0], ms[1]), fmaxf(ms[2], ms[3]));
    M = fmaxf(M, ror8(M));
    M = swap16_max(M); M = swap32_max(M);
    float Ls = 0.f;
#pragma unroll
    for (int k = 0; k < 4; ++k) { wt[k] = __builtin_amdgcn_exp2f(ms[k] - M); Ls += wt[k] * val2(ML[k][1], ML[k][3]); }
    Ls = swap32_add(Ls, Ls); Ls = swap16_add(Ls, Ls); Ls += ror8(Ls);
    const float inv = 1.0f / Ls;
#pragma unroll
    for (int k = 0; k < 4; ++k) { wt[k] *= inv; asm volatile("" : "+v"(wt[k])); }
    PIN();
    settle<4>(rs, off, xx, tg);
    float o[4];
#pragma unroll
    for (int e = 0; e < 4; ++e) o[e] = (wt[0] * val(xx[0][e]) + wt[1] * val(xx[1][e])) + (wt[2] * val(xx[2][e]) + wt[3] * val(xx[3][e]));
    const unsigned pk = cvtpk(red4_hi(o), 0.f);
    if ((lane & 8) == 0) L.ov[wv][4 * (lane & 7) + 2 * (lane >> 5) + ((lane >> 4) & 1)] = (unsigned short)pk;
  } else if (oproj) {
#pragma unroll
    for (int e = 0; e < 8; ++e)
#pragma unroll
      for (int u = 0; u < 4; ++u) BW[e][u] = __builtin_amdgcn_perm(WO[2 * u + 1][e >> 1], WO[2 * u][e >> 1], (e & 1) ? 0x07060302u : 0x05040100u);
#pragma unroll
    for (int e = 0; e < 8; ++e) asm volatile("" : "+v"(BW[e]));
  }
  PIN();
  __syncthreads();
  if (oproj) {
    const v4u ao = *(const v4u*)&L.ov[(a >> 2) & 1][8 * g];
    f32x4 d[8];
#pragma unroll
    for (int e = 0; e < 8; ++e) d[e] = mfma(ao, BW[e], f32x4{0.f, 0.f, 0.f, 0.f});
    const bool hi = g >> 1;
    put4(rs, WS_P2 + rb * 8192 + (g & 1) * 4096 + cb * 512 + a * 32 + (g >> 1) * 16,
         tag4(hi ? d[4][0] : d[0][0], hi ? d[5][0] : d[1][0], hi ? d[6][0] : d[2][0], hi ? d[7][0] : d[3][0], tg));
  }
  PIN();
  if ((w & 1) && w < 128) {
    const int o0 = WS_P2 + (16 * wv + (lane >> 3)) * 8192 + rb * 128 + (lane & 7) * 16;
    const int of2[2] = {o0, o0 + 8 * 8192};
    __builtin_amdgcn_s_sleep(NAP2);
    v4u yy[2] = {get4(rs, of2[0]), get4(rs, of2[1])};
    settle<2>(rs, of2, yy, tg);
    float v[4];
#pragma unroll
    for (int e = 0; e < 4; ++e) v[e] = val(yy[0][e]) + val(yy[1][e]);
    const float z = red4_hi(v);
    if ((lane & 8) == 0) L.fin[wv][4 * (lane & 7) + 2 * (lane >> 5) + ((lane >> 4) & 1)] = z;
    __syncthreads();
    if (t < 16) {
      f32x2 sm = *(const f32x2*)&L.fin[0][2 * t];
#pragma unroll
      for (int ww = 1; ww < NWAVE; ++ww) sm += *(const f32x2*)&L.fin[ww][2 * t];
      ((unsigned*)out)[rb * 16 + t] = cvtpk(sm[0], sm[1]);
    }
    if (w == 1 && t == 0) __builtin_amdgcn_raw_buffer_store_b64(v2u{tg[0] | (tg[1] << 10), tg[2] | (tg[3] << 10)}, rs, WS_EPOCH, 0, AUX_SC1);
  }
}

extern "C" void kernel_launch(void* const* d_in, const int* in_sizes, int n_in, void* d_out, int out_size, void* d_ws,
                              size_t ws_size, hipStream_t stream) {
  constexpr int kLds = 96 * 1024;
  static_assert(sizeof(Lds) <= kLds, "LDS");
  (void)hipFuncSetAttribute((const void*)gqa_decode_fused, hipFuncAttributeMaxDynamicSharedMemorySize, kLds);
  gqa_decode_fused<<<NWG, NTHR, kLds, stream>>>((const unsigned short*)d_in[0], (const unsigned short*)d_in[1],
                                                (const unsigned short*)d_in[2], (const unsigned short*)d_in[3],
                                                (const unsigned short*)d_in[4], (unsigned short*)d_out, (unsigned char*)d_ws);
}
```
